```python
import torch

def _softmax(x, axis=-1):
    return torch.softmax(x, dim=axis)
def _gelu(x):
    return torch.nn.functional.gelu(x, approximate="tanh")
def _silu(x):
    return torch.nn.functional.silu(x)
def _sigmoid(x):
    return torch.sigmoid(x)
def _layernorm(x, axis=-1, eps=1e-5):
    m = x.mean(dim=axis, keepdim=True)
    v = x.var(dim=axis, keepdim=True, unbiased=False)
    return (x - m) / torch.sqrt(v + eps)
def _scatter_add(tgt, idx, src, axis=0):
    return tgt.clone().index_add_(axis, idx, src)
def _segment_sum(src, seg, n):
    out = torch.zeros((n,) + src.shape[1:], dtype=src.dtype)
    return out.index_add_(0, seg, src)
def _pad(x, pw):
    flat = tuple(v for pair in reversed(pw) for v in pair)
    return torch.nn.functional.pad(x, flat)

kernel_name = 'standalone__convnext_block__vision'

def setup_inputs():

    _w = (lambda a: (lambda t: t.to(torch.bfloat16) if t.is_floating_point() else t)(torch.as_tensor(a)))
    H = W = 224
    C = 96
    return {
        "x": _w((lambda *s: torch.randn(*s, dtype=torch.bfloat16) * 0.05)(1, H, W, C)),
        "dw": _w((lambda *s: torch.randn(*s, dtype=torch.bfloat16) * 0.05)(7, 7, C)),
        "w1": _w((lambda *s: torch.randn(*s, dtype=torch.bfloat16) * 0.05)(C, 4 * C)),
        "w2": _w((lambda *s: torch.randn(*s, dtype=torch.bfloat16) * 0.05)(4 * C, C)),
    }

def reference(x, dw, w1, w2):
    B, H, W, C = x.shape
    pad = 3
    xp = _pad(x, ((0,0),(pad,pad),(pad,pad),(0,0)))
    out = torch.zeros_like(x)
    for i in range(7):
        for j in range(7):
            out = out + xp[:, i:i+H, j:j+W, :] * dw[i, j][None, None, None, :]
    h = _layernorm(out, axis=-1)
    return x + _gelu(h @ w1) @ w2
```

```cpp
#include <hip/hip_runtime.h>
#include <hip/hip_bf16.h>

static __device__ __forceinline__ float bf2f(unsigned short u) { return __uint_as_float(((unsigned)u) << 16); }
static __device__ __forceinline__ unsigned short f2bf(float f) { __hip_bfloat16 b = __float2bfloat16(f); return *(unsigned short*)&b; }

__global__ __launch_bounds__(384) void naive_kernel(const unsigned short* __restrict__ x, const unsigned short* __restrict__ dw,
                             const unsigned short* __restrict__ w1, const unsigned short* __restrict__ w2,
                             unsigned short* __restrict__ out) {
    __shared__ float sh[8][96];
    __shared__ float sg[8][384];
    const int H = 224, W = 224, C = 96;
    int p0 = blockIdx.x * 8;
    int t = threadIdx.x;
    for (int idx = t; idx < 8 * 96; idx += 384) {
        int pl = idx / 96, c = idx % 96;
        int p = p0 + pl; int h = p / W, w = p % W;
        float acc = 0.f;
        for (int i = 0; i < 7; i++) for (int j = 0; j < 7; j++) {
            int hh = h + i - 3, ww = w + j - 3;
            if (hh >= 0 && hh < H && ww >= 0 && ww < W) acc += bf2f(x[(hh * W + ww) * C + c]) * bf2f(dw[(i * 7 + j) * C + c]);
        }
        sh[pl][c] = acc;
    }
    __syncthreads();
    if (t < 8) {
        float m = 0; for (int c = 0; c < 96; c++) m += sh[t][c]; m /= 96.f;
        float v = 0; for (int c = 0; c < 96; c++) { float d = sh[t][c] - m; v += d * d; } v /= 96.f;
        float r = 1.f / sqrtf(v + 1e-5f);
        for (int c = 0; c < 96; c++) sh[t][c] = bf2f(f2bf((sh[t][c] - m) * r));
    }
    __syncthreads();
    {
        float acc[8]; for (int i = 0; i < 8; i++) acc[i] = 0;
        for (int c = 0; c < 96; c++) { float wv = bf2f(w1[c * 384 + t]); for (int i = 0; i < 8; i++) acc[i] += sh[i][c] * wv; }
        for (int i = 0; i < 8; i++) { float u = acc[i]; float g = 0.5f * u * (1.f + tanhf(0.7978845608f * (u + 0.044715f * u * u * u))); sg[i][t] = bf2f(f2bf(g)); }
    }
    __syncthreads();
    for (int idx = t; idx < 8 * 96; idx += 384) {
        int pl = idx / 96, c = idx % 96;
        float acc = 0; for (int k = 0; k < 384; k++) acc += sg[pl][k] * bf2f(w2[k * 96 + c]);
        int p = p0 + pl;
        out[p * C + c] = f2bf(bf2f(x[p * C + c]) + acc);
    }
}

extern "C" void kernel_launch(void* const* d_in, const int* in_sizes, int n_in,
                              void* d_out, int out_size, void* d_ws, size_t ws_size,
                              hipStream_t stream) {
    naive_kernel<<<224 * 224 / 8, 384, 0, stream>>>((const unsigned short*)d_in[0], (const unsigned short*)d_in[1],
        (const unsigned short*)d_in[2], (const unsigned short*)d_in[3], (unsigned short*)d_out);
}
```

```cpp
#include <hip/hip_runtime.h>

typedef __attribute__((ext_vector_type(8))) __bf16 bf16x8;
typedef __attribute__((ext_vector_type(2))) __bf16 bf16x2;
typedef __attribute__((ext_vector_type(4))) short s16x4;
typedef __attribute__((ext_vector_type(16))) float f32x16;
typedef __attribute__((ext_vector_type(4))) float f32x4;
typedef __attribute__((ext_vector_type(2))) float f32x2;
typedef __attribute__((ext_vector_type(4))) unsigned u32x4;
typedef __attribute__((ext_vector_type(2))) unsigned u32x2;
typedef __attribute__((ext_vector_type(4))) int i32x4;
#define NST 18
#define AS3 __attribute__((address_space(3)))
#define DI __device__ __forceinline__
#define PIN(x) asm volatile("" :: "v"(x))
#define SB() __builtin_amdgcn_sched_barrier(0)
#define STAUX 2

#define STAMP(i) do {} while (0)

namespace {
constexpr int IMG = 224, XBYTES = IMG * IMG * 192;
constexpr int TBL_PS = 1128;
constexpr int TBL = 128;
constexpr int XP = 54272;
constexpr int SP = 147456;
constexpr int FS = 122880;
constexpr int CNT = 163840 - 64;
constexpr int H0 = CNT - 39264;
constexpr int W2B = 73728;
constexpr float GA = 0.46866869f;
constexpr float GC = 4.91222954f;
}

__shared__ __attribute__((aligned(16))) char smem[163840];

template <typename T> DI AS3 T* lp(unsigned off) { return (AS3 T*)((AS3 char*)smem + off); }
DI unsigned pk_bf16(float a, float b) { f32x2 v = {a, b}; return __builtin_bit_cast(unsigned, __builtin_convertvector(v, bf16x2)); }
DI f32x16 mfma32(u32x4 a, u32x4 b, f32x16 c) { return __builtin_amdgcn_mfma_f32_32x32x16_bf16(__builtin_bit_cast(bf16x8, a), __builtin_bit_cast(bf16x8, b), c, 0, 0, 0); }
DI f32x4 mfma16(u32x4 a, u32x4 b, f32x4 c) { return __builtin_amdgcn_mfma_f32_16x16x32_bf16(__builtin_bit_cast(bf16x8, a), __builtin_bit_cast(bf16x8, b), c, 0, 0, 0); }
DI u32x2 trread(unsigned off) { return __builtin_bit_cast(u32x2, __builtin_amdgcn_ds_read_tr16_b64_v4i16(lp<s16x4>(off))); }
DI u32x4 trfrag(unsigned off0, unsigned off1) { u32x2 a = trread(off0), b = trread(off1); return u32x4{a.x, a.y, b.x, b.y}; }
template <typename T> DI T ldv(unsigned off) { return *(volatile AS3 T*)lp<T>(off); }
template <typename T> DI void stv(unsigned off, T v) { *(volatile AS3 T*)lp<T>(off) = v; }
DI u32x4 rd2x64(unsigned off0, unsigned off1) { u32x2 a = ldv<u32x2>(off0), b = ldv<u32x2>(off1); return u32x4{a.x, a.y, b.x, b.y}; }
DI void lgkm0() { __builtin_amdgcn_s_waitcnt(0xC07F); }
DI void barrier() { asm volatile("s_waitcnt lgkmcnt(0)\n\ts_barrier" ::: "memory"); }
DI void dma(i32x4 rs, unsigned voff, unsigned soff, unsigned ldsdst) {
    asm volatile("s_mov_b32 m0, %3\n\ts_nop 0\n\tbuffer_load_dwordx4 %0, %1, %2 offen lds" :: "v"(voff), "s"(rs), "s"(soff), "s"(ldsdst) : "memory");
}
DI i32x4 mkrs(const void* p, int bytes) { i32x4 r; r.x = (int)(size_t)p; r.y = (int)((size_t)p >> 32) & 0xffff; r.z = bytes; r.w = 0x00020000; return r; }

struct Mlp {
    u32x4 h[6];
    f32x16 acc[3];
    u32x4 w1[6], w2[6];
    f32x16 X[3], Z[3];
    u32x4 G[2];
    unsigned b1, b2;
};

DI void load_w1(Mlp& m, unsigned a) {
#pragma unroll
    for (int s = 0; s < 6; s++) m.w1[s] = trfrag(a + 1024 * s, a + 1024 * s + 256);
}
DI void load_w2(Mlp& m, unsigned a) {
#pragma unroll
    for (int s = 0; s < 2; s++)
#pragma unroll
        for (int ct = 0; ct < 3; ct++) m.w2[s * 3 + ct] = trfrag(a + 3072 * s + 64 * ct, a + 3072 * s + 1536 + 64 * ct);
}

template <int K, bool G1, bool EXP, bool RCP, bool G2, bool L1, bool L2>
DI void step(Mlp& m, unsigned j1, unsigned j2) {
    constexpr int KE = (K + 2) % 3, KR = (K + 1) % 3;
    int te = 0;
    auto trans = [&](int n) {
#pragma unroll
        for (int q = 0; q < n; q++) {
            int t = te++;
            if (t < 16) { if (EXP) { float v = __builtin_amdgcn_exp2f(-m.Z[KE][t]); PIN(v); m.Z[KE][t] = v; } }
            else if (t < 32) { if (RCP) { float v = __builtin_amdgcn_rcpf(m.Z[KR][t - 16]); PIN(v); m.Z[KR][t - 16] = v; } }
        }
    };
    const f32x16 zero = {};
#pragma unroll
    for (int g = 0; g < 12; g++) {
        if (g < 6) { if (G1) m.X[K] = mfma32(m.w1[g], m.h[g], g == 0 ? zero : m.X[K]); }
        else if (G2) { int s = (g - 6) / 3, ct = (g - 6) % 3; m.acc[ct] = mfma32(m.w2[s * 3 + ct], m.G[s], m.acc[ct]); }
        SB();
        if (g < 4) {
            if (G2) {
                m.G[g >> 1][(g & 1) * 2] = pk_bf16(m.Z[K][4 * g], m.Z[K][4 * g + 1]);
                m.G[g >> 1][(g & 1) * 2 + 1] = pk_bf16(m.Z[K][4 * g + 2], m.Z[K][4 * g + 3]);
            }
            trans(2);
        } else trans(3);
        SB();
    }
    if (L1) load_w1(m, j1);
    if (L2) load_w2(m, j2);
    SB();
    const f32x2 ca = {GA, GA}, cc = {GC, GC};
#pragma unroll
    for (int q = 0; q < 8; q++) {
        if (EXP) { f32x2 e = {m.Z[KE][2 * q], m.Z[KE][2 * q + 1]}; e = __builtin_elementwise_fma(e, ca, ca); PIN(e); m.Z[KE][2 * q] = e.x; m.Z[KE][2 * q + 1] = e.y; }
    }
#pragma unroll
    for (int q = 0; q < 8; q++) {
        if (RCP) { f32x2 r = {m.Z[KR][2 * q], m.Z[KR][2 * q + 1]}, x = {m.X[KR][2 * q], m.X[KR][2 * q + 1]}; r = r * x; PIN(r); m.Z[KR][2 * q] = r.x; m.Z[KR][2 * q + 1] = r.y; }
    }
#pragma unroll
    for (int q = 0; q < 8; q++) {
        if (G1) { f32x2 x = {m.X[K][2 * q], m.X[K][2 * q + 1]}; f32x2 t = __builtin_elementwise_fma(x, x, cc); t = t * x; PIN(t); m.Z[K][2 * q] = t.x; m.Z[K][2 * q + 1] = t.y; }
    }
    lgkm0();
    SB();
}

DI void wait_cnt(unsigned off, unsigned target) {
    while (__builtin_amdgcn_readfirstlane(*(volatile AS3 unsigned*)lp<unsigned>(off)) < target) __builtin_amdgcn_s_sleep(1);
    asm volatile("" ::: "memory");
}
DI void signal_cnt(unsigned off, int lane) {
    asm volatile("s_waitcnt lgkmcnt(0)" ::: "memory");
    if (lane == 0) __hip_atomic_fetch_add(lp<unsigned>(off), 1u, __ATOMIC_RELAXED, __HIP_MEMORY_SCOPE_WORKGROUP);
    asm volatile("" ::: "memory");
}
DI unsigned haddr(int p) { return H0 + p * 200 + (p / 28) * 8; }

#define OPQ(x) asm volatile("" : "+v"(x))
DI i32x4 row_rs(const char* x, int gy) {
    const char* p = x + (long)gy * (IMG * 192);
    return mkrs(p, (unsigned)gy < (unsigned)IMG ? IMG * 192 : 0);
}
DI void xload_row(u32x4 (&d)[4], const unsigned (&c)[4], i32x4 rs) {
    asm volatile("buffer_load_dwordx4 %0, %4, %8, 0 offen\n\tbuffer_load_dwordx4 %1, %5, %8, 0 offen\n\t"
                 "buffer_load_dwordx4 %2, %6, %8, 0 offen\n\tbuffer_load_dwordx4 %3, %7, %8, 0 offen"
                 : "=&v"(d[0]), "=&v"(d[1]), "=&v"(d[2]), "=&v"(d[3]) : "v"(c[0]), "v"(c[1]), "v"(c[2]), "v"(c[3]), "s"(rs) : "memory");
}
DI void xload_row_hi(u32x4 (&d)[4], const unsigned (&c)[4], i32x4 rs, int w) {
    asm volatile("s_cmp_lt_u32 %9, 4\n\ts_cbranch_scc1 .Lskip%=\n\t"
                 "buffer_load_dwordx4 %0, %4, %8, 0 offen\n\tbuffer_load_dwordx4 %1, %5, %8, 0 offen\n\t"
                 "buffer_load_dwordx4 %2, %6, %8, 0 offen\n\tbuffer_load_dwordx4 %3, %7, %8, 0 offen\n.Lskip%=:"
                 : "=&v"(d[0]), "=&v"(d[1]), "=&v"(d[2]), "=&v"(d[3]) : "v"(c[0]), "v"(c[1]), "v"(c[2]), "v"(c[3]), "s"(rs), "s"(w) : "memory", "scc");
}
template <int N> DI void xwait(u32x4 (&d)[4]) { asm volatile("s_waitcnt vmcnt(%c4)" : "+v"(d[0]), "+v"(d[1]), "+v"(d[2]), "+v"(d[3]) : "i"(N) : "memory"); }
DI void xpose_row(const u32x4 (&v)[4], unsigned a) {
#pragma unroll
    for (int d = 0; d < 4; d++) {
        u32x2 lo = {__builtin_amdgcn_perm(v[1][d], v[0][d], 0x05040100u), __builtin_amdgcn_perm(v[3][d], v[2][d], 0x05040100u)};
        u32x2 hi = {__builtin_amdgcn_perm(v[1][d], v[0][d], 0x07060302u), __builtin_amdgcn_perm(v[3][d], v[2][d], 0x07060302u)};
        stv<u32x2>(a + (2 * d) * 960, lo);
        stv<u32x2>(a + (2 * d + 1) * 960, hi);
    }
}

__global__ __launch_bounds__(512) void convnext_kernel(const char* __restrict__ x, const char* __restrict__ dw, const char* __restrict__ w1,
                                                       const char* __restrict__ w2, char* __restrict__ out
) {
    STAMP(0);
    const int tid = threadIdx.x, lane = tid & 63, w = __builtin_amdgcn_readfirstlane(tid >> 6);
    const int b = blockIdx.x, xcd = b & 7, bi = b >> 3;
    const int ty = (xcd >> 1) * 4 + (bi >> 3), tx = (xcd & 1) * 8 + (bi & 7);
    const i32x4 rsx = mkrs(x, XBYTES), rsd = mkrs(dw, 49 * 192);

    unsigned D[7];
    const int ti = tid / 48, tcp = tid % 48;
    const unsigned doff = ti * 1344 + 4 * tcp;
    const int cgp = lane / 12, cg = lane % 12;
    unsigned coff[4];
#pragma unroll
    for (int i = 0; i < 4; i++) { coff[i] = (tx * 14 - 3 + 4 * cgp + i) * 192 + cg * 16; PIN(coff[i]); }
    PIN(doff);
    asm volatile("s_cmp_ge_u32 %9, 6\n\ts_cbranch_scc1 .Lskip%=\n\t"
                 "buffer_load_dword %0, %7, %8, 0 offen\n\tbuffer_load_dword %1, %7, %8, 0 offen offset:192\n\tbuffer_load_dword %2, %7, %8, 0 offen offset:384\n\t"
                 "buffer_load_dword %3, %7, %8, 0 offen offset:576\n\tbuffer_load_dword %4, %7, %8, 0 offen offset:768\n\tbuffer_load_dword %5, %7, %8, 0 offen offset:960\n\t"
                 "buffer_load_dword %6, %7, %8, 0 offen offset:1152\n.Lskip%=:"
                 : "=&v"(D[0]), "=&v"(D[1]), "=&v"(D[2]), "=&v"(D[3]), "=&v"(D[4]), "=&v"(D[5]), "=&v"(D[6]) : "v"(doff), "s"(rsd), "s"(w) : "memory", "scc");
    u32x4 xv0[4], xv1[4], xv2[4];
    const int gy0 = ty * 14 - 3 + w;
    xload_row(xv0, coff, row_rs(x, gy0));
    xload_row_hi(xv2, coff, row_rs(x, gy0 + 12), w);
    STAMP(1);
    if (tid < 480) {
        unsigned a = XP + 40 + 48 * tid + 16 * (tid / 160);
#pragma unroll
        for (int k = 0; k < 4; k++) stv<u32x2>(a + k * 23088, u32x2{0, 0});
    }
    const int g4 = lane >> 4, n16 = lane & 15;
    unsigned aoff;
    bool av[4][2];
    {
        int py = n16 >> 3, px = n16 & 7, ry = g4 >> 1, xh = g4 & 1;
        aoff = 6 * w * TBL_PS + (ry - py) * 80 + (8 * xh - px + 3) * 8 + TBL;
        OPQ(aoff);
#pragma unroll
        for (int s = 0; s < 4; s++)
#pragma unroll
            for (int q = 0; q < 2; q++) {
                int i = 2 * s + ry - py, u = 8 * xh - px + 4 * q;
                av[s][q] = i >= 0 && i <= 6 && u >= -3 && u <= 6;
            }
    }
    u32x2 Ah[12][4][2];
#pragma unroll
    for (int cl = 0; cl < 9; cl++)
#pragma unroll
        for (int s = 0; s < 4; s++)
#pragma unroll
            for (int q = 0; q < 2; q++) { Ah[cl][s][q] = u32x2{0, 0}; OPQ(Ah[cl][s][q]); }
    auto load_A = [&](int c0, int c1) {
#pragma unroll
        for (int q = 0; q < 2; q++)
#pragma unroll
            for (int sg = 0; sg < 3; sg++)
                if (av[sg == 2 ? 3 : sg][q]) {
#pragma unroll
                    for (int s = (sg == 2 ? 3 : sg); s <= (sg == 0 ? 0 : sg + 1); s++)
#pragma unroll
                        for (int cl = c0; cl < c1; cl++) Ah[cl][s][q] = ldv<u32x2>(aoff + (cl >> 1) * TBL_PS + (cl & 1) * 560 + 160 * s + 32 * q);
                }
    };
    unsigned xa = XP + cg * (8 * 960 + 16) + (w & 1) * 480 + (w >> 1) * 48 + cgp * 8;
    unsigned ta = TBL + tcp * TBL_PS + ti * 80;
    OPQ(xa); OPQ(ta);
    asm volatile("s_cmp_ge_u32 %7, 4\n\ts_cbranch_scc1 .Lw%=\n\ts_waitcnt vmcnt(4)\n.Lw%=:\n\ts_waitcnt vmcnt(8)"
                 : "+v"(D[0]), "+v"(D[1]), "+v"(D[2]), "+v"(D[3]), "+v"(D[4]), "+v"(D[5]), "+v"(D[6]) : "s"(w) : "memory", "scc");
    STAMP(2);
    if (tid < 336) {
#pragma unroll
        for (int hi = 0; hi < 2; hi++) {
            unsigned P[12];
            P[0] = 0; P[1] = 0; P[10] = 0; P[11] = 0;
            P[2] = hi ? (D[0] & 0xffff0000u) : (D[0] << 16);
#pragma unroll
            for (int v = 0; v < 6; v++) P[v + 3] = __builtin_amdgcn_perm(D[v + 1], D[v], hi ? 0x07060302u : 0x05040100u);
            P[9] = hi ? (D[6] >> 16) : (D[6] & 0xffffu);
#pragma unroll
            for (int u = 0; u < 10; u++) stv<u32x2>(ta + hi * 560 + 8 * u, u32x2{P[u], P[u + 2]});
        }
    }
    xload_row(xv1, coff, row_rs(x, gy0 + 8));
    STAMP(3);
    barrier();
    STAMP(4);
    load_A(0, 9);
    SB();
    STAMP(5);
    {
        asm volatile("s_cmp_ge_u32 %4, 4\n\ts_cbranch_scc1 .Lw%=\n\ts_waitcnt vmcnt(4)\n.Lw%=:\n\ts_waitcnt vmcnt(8)"
                     : "+v"(xv0[0]), "+v"(xv0[1]), "+v"(xv0[2]), "+v"(xv0[3]) : "s"(w) : "memory", "scc");
        xpose_row(xv0, xa);
        SB();
        xwait<4>(xv2);
        if (w >= 4) xpose_row(xv2, xa + 6 * 48);
        SB();
#pragma unroll
        for (int cl = 9; cl < 12; cl++)
#pragma unroll
            for (int s = 0; s < 4; s++)
#pragma unroll
                for (int q = 0; q < 2; q++) { Ah[cl][s][q] = u32x2{0, 0}; OPQ(Ah[cl][s][q]); }
        load_A(9, 12);
        SB();
        STAMP(6);
        xwait<0>(xv1);
        STAMP(7);
        xpose_row(xv1, xa + 4 * 48);
        SB();
    }
    const i32x4 rw1 = mkrs(w1, 96 * 768), rw2 = mkrs(w2, 384 * 192);
    const unsigned v1 = (lane >> 2) * 768 + (lane & 3) * 16;
    const bool inN0 = n16 < 4 || n16 >= 12;
    const int idx8 = n16 < 4 ? n16 : (n16 >= 12 ? n16 - 8 : n16 - 4);
    const int hb = idx8 < 7 ? idx8 : 6;
    const int wb = (inN0 == (hb != 1 && hb != 6)) ? 1 : 0;
    const unsigned bl = XP + (g4 >> 1) * 480 + hb * 48 + (wb + (g4 & 1)) * 16 + 12 * w * 960;
    const unsigned bA = bl + ((12 * w) >> 3) * 16, bB = bl + ((12 * w + 4) >> 3) * 16;
    PIN(v1); PIN(bA); PIN(bB);
    STAMP(8);
    barrier();
    STAMP(9);
    f32x4 o[12];
    float s1[4], s2[4];
    {
        u32x4 B[12][4];
        auto baddr = [&](int cl, int s) { return (cl < 4 ? bA : (cl < 8 ? bB : bA + 16)) + cl * 960 + 48 * s; };
#pragma unroll
        for (int s = 0; s < 4; s++) B[0][s] = ldv<u32x4>(baddr(0, s));
#pragma unroll
        for (int s = 0; s < 4; s++) B[1][s] = ldv<u32x4>(baddr(1, s));
        SB();
#pragma unroll
        for (int cl = 0; cl < 12; cl++) {
            if (cl < 11) __builtin_amdgcn_s_waitcnt(0xC47F); else __builtin_amdgcn_s_waitcnt(0xC07F);
            SB();
#pragma unroll
            for (int s = 0; s < 4; s++) {
                const u32x4 a = {Ah[cl][s][0].x, Ah[cl][s][0].y, Ah[cl][s][1].x, Ah[cl][s][1].y};
                if (s == 0) asm volatile("v_mfma_f32_16x16x32_bf16 %0, %1, %2, 0" : "=&v"(o[cl]) : "v"(a), "v"(B[cl][s]));
                else asm volatile("v_mfma_f32_16x16x32_bf16 %0, %1, %2, %0" : "+v"(o[cl]) : "v"(a), "v"(B[cl][s]));
                SB();
                if (cl + 2 < 12) B[cl + 2][s] = ldv<u32x4>(baddr(cl + 2, s));
                if (s == 3 && (cl & 1) == 0) dma(rw1, v1, 64 * w + 12288 * (cl >> 1), 6144 * w + 1024 * (cl >> 1));
                SB();
                if (cl == 3) asm volatile("v_add_f32 %0, %1, %2" : "=&v"(s1[s]) : "v"(o[0][s]), "v"(o[1][s]));
                if (cl > 3) asm volatile("v_add_f32 %0, %0, %1" : "+v"(s1[s]) : "v"(o[cl - 2][s]));
                SB();
            }
        }
        asm volatile("s_nop 7" : "+v"(o[11]));
        f32x2 qa = {o[0][0], o[0][1]}, qb = {o[0][2], o[0][3]};
        qa *= qa; qb *= qb;
#pragma unroll
        for (int cl = 1; cl < 12; cl++) {
            f32x2 a = {o[cl][0], o[cl][1]}, c = {o[cl][2], o[cl][3]};
            qa = __builtin_elementwise_fma(a, a, qa); qb = __builtin_elementwise_fma(c, c, qb);
        }
        s2[0] = qa.x; s2[1] = qa.y; s2[2] = qb.x; s2[3] = qb.y;
#pragma unroll
        for (int e = 0; e < 4; e++) s1[e] += o[10][e] + o[11][e];
    }
    stv<f32x4>(SP + w * 2048 + lane * 16, f32x4{s1[0], s1[1], s1[2], s1[3]});
    stv<f32x4>(SP + w * 2048 + 1024 + lane * 16, f32x4{s2[0], s2[1], s2[2], s2[3]});
    const int hf = lane >> 5;
    unsigned fdst;
    {
        int slot = 32 * w + (lane & 31);
        int sl = slot >> 2, e = slot & 3, sn = sl & 15, sg = sl >> 4;
        int si = sn < 4 ? sn : (sn >= 12 ? sn - 8 : sn - 4);
        int swb = ((sn < 4 || sn >= 12) == (si != 1 && si != 6)) ? 1 : 0;
        int col = 8 * swb + 4 * (sg & 1) + e;
        int p = (si < 7 && col < 14) ? (2 * si + (sg >> 1)) * 14 + col : 199;
        fdst = FS + hf * 800 + p * 4;
        PIN(fdst);
    }
    const int col0 = 8 * wb + 4 * (g4 & 1);
    const int p0 = (2 * hb + (g4 >> 1)) * 14 + col0;
    const unsigned hdst = H0 + p0 * 200 + hb * 8 + 24 * w;
    PIN(hdst);
    const i32x4 rws = w < 6 ? rw2 : rw1;
    const unsigned vs = w < 6 ? lane * 16 : v1;
    const unsigned sstep = w < 6 ? 1024 : 12288;
    auto dma_stage = [&](int stage, int n) {
        int j = w < 6 ? w + 6 * stage : w + 2 + 2 * stage;
        dma(rws, vs, (w < 6 ? 6144 : 64) * j + sstep * n, (w < 6 ? W2B : 0) + 6144 * j + 1024 * n);
    };
    STAMP(10);
    barrier();
    {
        unsigned a = SP + hf * 1024 + (32 * w + (lane & 31)) * 4;
        float t[8];
#pragma unroll
        for (int q = 0; q < 8; q++) t[q] = *lp<float>(a + 2048 * q);
        float T = ((t[0] + t[1]) + (t[2] + t[3])) + ((t[4] + t[5]) + (t[6] + t[7]));
        auto sw = __builtin_amdgcn_permlane32_swap(__float_as_uint(T), __float_as_uint(T), false, false);
        const unsigned swa = sw[0], swb2 = sw[1];
        float mean = __uint_as_float(swa) * (1.f / 96.f);
        float var = __builtin_fmaf(-mean, mean, __uint_as_float(swb2) * (1.f / 96.f));
        var = var > 0.f ? var : 0.f;
        float r = GA * __builtin_amdgcn_rsqf(var + 1e-5f);
        stv<float>(fdst, hf ? -mean * r : r);
    }
    barrier();
    STAMP(11);
    {
        f32x2 ra = ldv<f32x2>(FS + p0 * 4), rb = ldv<f32x2>(FS + p0 * 4 + 8);
        f32x2 ma = ldv<f32x2>(FS + 800 + p0 * 4), mb = ldv<f32x2>(FS + 800 + p0 * 4 + 8);
        if (tid < 16) stv<unsigned>(CNT + 4 * tid, 0);
        f32x2 na[12], nb[12];
#pragma unroll
        for (int cl = 0; cl < 12; cl++) {
            na[cl] = __builtin_elementwise_fma(f32x2{o[cl][0], o[cl][1]}, ra, ma);
            nb[cl] = __builtin_elementwise_fma(f32x2{o[cl][2], o[cl][3]}, rb, mb);
        }
        if (idx8 < 7) {
#pragma unroll
            for (int q = 0; q < 3; q++) {
                stv<u32x2>(hdst + 8 * q, u32x2{pk_bf16(na[4 * q].x, na[4 * q + 1].x), pk_bf16(na[4 * q + 2].x, na[4 * q + 3].x)});
                stv<u32x2>(hdst + 200 + 8 * q, u32x2{pk_bf16(na[4 * q].y, na[4 * q + 1].y), pk_bf16(na[4 * q + 2].y, na[4 * q + 3].y)});
            }
            if (col0 < 12) {
#pragma unroll
                for (int q = 0; q < 3; q++) {
                    stv<u32x2>(hdst + 400 + 8 * q, u32x2{pk_bf16(nb[4 * q].x, nb[4 * q + 1].x), pk_bf16(nb[4 * q + 2].x, nb[4 * q + 3].x)});
                    stv<u32x2>(hdst + 600 + 8 * q, u32x2{pk_bf16(nb[4 * q].y, nb[4 * q + 1].y), pk_bf16(nb[4 * q + 2].y, nb[4 * q + 3].y)});
                }
            }
        }
    }
    Mlp m;
    {
        int q = (lane >> 2) & 3, p = lane & 3, gm = (lane >> 4) & 1;
        m.b1 = 512 * hf + 64 * q + 32 * gm + 8 * p;
        m.b2 = W2B + (4 * hf + q) * 192 + 32 * (p & 1) + 16 * gm + 8 * (p >> 1);
    }
    const int tile = w < 7 ? w : 4;
    auto hrow = [&](int t) { int p = t * 32 + (lane & 31); p = p < 196 ? p : 195; return haddr(p); };
    const unsigned hsrc = hrow(tile) + 16 * hf;
    PIN(hsrc); PIN(m.b1); PIN(m.b2);
    asm volatile("s_waitcnt vmcnt(0)" ::: "memory");
    barrier();
    STAMP(12);
    auto load_h = [&](unsigned a) {
#pragma unroll
        for (int s = 0; s < 6; s++) m.h[s] = rd2x64(a + 32 * s, a + 32 * s + 8);
    };
    const int skip = (w >= 4 && w < 7) ? w - 4 : 99;
    const int NG = w < 4 ? 4 : 3;
    auto grp = [&](int g) { return 18432 * (g + (g >= skip ? 1 : 0)); };
    load_h(hsrc);
    load_w1(m, m.b1 + grp(0));
    {
        const u32x4 z4 = {}; const f32x16 zero = {};
#pragma unroll
        for (int ct = 0; ct < 3; ct++) m.acc[ct] = mfma32(z4, z4, zero);
    }
    {
        unsigned a1 = m.b1 + grp(0);
        dma_stage(0, 0); dma_stage(0, 1); dma_stage(0, 2);
        step<0, true, false, false, false, true, false>(m, a1 + 6144, 0);
        dma_stage(0, 3); dma_stage(0, 4); dma_stage(0, 5);
        step<1, true, true, false, false, true, false>(m, a1 + 12288, 0);
        asm volatile("s_waitcnt vmcnt(0)" ::: "memory");
        barrier();
        STAMP(13);
        dma_stage(1, 0); dma_stage(1, 1); dma_stage(1, 2);
        step<2, true, true, true, false, true, true>(m, m.b1 + grp(1), m.b2 + grp(0));
        dma_stage(1, 3); dma_stage(1, 4); dma_stage(1, 5);
        if (w == 7) load_h(hrow(5) + 16 * hf);
    }
    STAMP(14);
    auto flush_partial = [&](int t) {
        if (t * 32 + (lane & 31) < 196) {
            unsigned a = hrow(t) + 32 * hf;
#pragma unroll
            for (int ct = 0; ct < 3; ct++)
#pragma unroll
                for (int q = 0; q < 4; q++)
                    stv<u32x2>(a + 64 * ct + 8 * q, u32x2{pk_bf16(m.acc[ct][4 * q], m.acc[ct][4 * q + 1]), pk_bf16(m.acc[ct][4 * q + 2], m.acc[ct][4 * q + 3])});
        }
        signal_cnt(CNT + 8 + 4 * (t - 4), lane);
    };
#pragma unroll 1
    for (int g = 1; g < NG; g++) {
        int j = grp(g), jn = g + 1 < NG ? grp(g + 1) : 0;
        unsigned a1 = m.b1 + j, a2 = m.b2 + grp(g - 1);
        step<0, true, true, true, true, true, true>(m, a1 + 6144, a2 + 6144);
        if (g == 1) { asm volatile("s_waitcnt vmcnt(0)" ::: "memory"); signal_cnt(CNT, lane); }
        step<1, true, true, true, true, true, true>(m, a1 + 12288, a2 + 12288);
        if (g == (skip < 99 ? 1 : 2)) wait_cnt(CNT, 8);
        step<2, true, true, true, true, true, true>(m, m.b1 + jn, m.b2 + j);
        if (g == 1) signal_cnt(CNT + 4, lane);
        if (w == 7) {
            if (g == 1) load_h(hrow(6) + 16 * hf);
            flush_partial(3 + g);
            const u32x4 z4 = {}; const f32x16 zero = {};
#pragma unroll
            for (int ct = 0; ct < 3; ct++) m.acc[ct] = mfma32(z4, z4, zero);
        }
    }
    const unsigned scr = w < 6 ? 6144 * w : SP;
    unsigned goff[2];
    if (w < 7) {
        wait_cnt(CNT + 4, 8);
#pragma unroll
        for (int hh = 0; hh < 2; hh++) {
            int px = (lane >> 2) + 16 * hh, p = 32 * w + px;
            int swz = ((px >> 1) & 3) ^ ((px >> 3) & 1);
            goff[hh] = p < 196 ? ((ty * 14 + p / 14) * IMG + tx * 14 + p % 14) * 192 + 16 * ((lane & 3) ^ swz) : 0x80000000u;
            if (p < 196) {
#pragma unroll
                for (int ct = 0; ct < 3; ct++) dma(rsx, goff[hh], 64 * ct, scr + 3072 * hh + 1024 * ct);
            }
        }
    }
    {
        unsigned a2 = m.b2 + grp(NG - 1);
        step<0, false, true, true, true, false, true>(m, 0, a2 + 6144);
        step<1, false, false, true, true, false, true>(m, 0, a2 + 12288);
        step<2, false, false, false, true, false, false>(m, 0, 0);
    }
    STAMP(15);
    if (w == 7) {
        flush_partial(6);
    } else {
        u32x4 id[2];
        {
            int row = lane & 31, r = (row & 3) + 4 * (row >> 3);
            bool mine = ((row >> 2) & 1) == hf;
#pragma unroll
            for (int u = 0; u < 2; u++)
#pragma unroll
                for (int d = 0; d < 4; d++) id[u][d] = (mine && (r >> 1) == 4 * u + d) ? ((r & 1) ? 0x3f800000u : 0x00003f80u) : 0u;
        }
        const int px = lane & 31, swz = ((px >> 1) & 3) ^ ((px >> 3) & 1);
        const unsigned sa = scr + 3072 * (px >> 4) + 64 * (px & 15);
        asm volatile("s_waitcnt vmcnt(0)" ::: "memory");
#pragma unroll
        for (int u = 0; u < 2; u++)
#pragma unroll
            for (int ct = 0; ct < 3; ct++) m.acc[ct] = mfma32(id[u], ldv<u32x4>(sa + 16 * ((2 * hf + u) ^ swz) + 1024 * ct), m.acc[ct]);
        if (w >= 4) {
            wait_cnt(CNT + 8 + 4 * (w - 4), 1);
            unsigned a = hrow(w) + 32 * hf;
#pragma unroll
            for (int u = 0; u < 2; u++)
#pragma unroll
                for (int ct = 0; ct < 3; ct++) m.acc[ct] = mfma32(id[u], rd2x64(a + 64 * ct + 16 * u, a + 64 * ct + 16 * u + 8), m.acc[ct]);
        }
        if (32 * w + px < 196) {
#pragma unroll
            for (int u = 0; u < 2; u++)
#pragma unroll
                for (int ct = 0; ct < 3; ct++)
                    stv<u32x4>(sa + 16 * ((2 * hf + u) ^ swz) + 1024 * ct,
                               u32x4{pk_bf16(m.acc[ct][8 * u], m.acc[ct][8 * u + 1]), pk_bf16(m.acc[ct][8 * u + 2], m.acc[ct][8 * u + 3]),
                                     pk_bf16(m.acc[ct][8 * u + 4], m.acc[ct][8 * u + 5]), pk_bf16(m.acc[ct][8 * u + 6], m.acc[ct][8 * u + 7])});
        }
        auto rso = __builtin_amdgcn_make_buffer_rsrc((void*)out, 0, XBYTES, 0x00020000);
#pragma unroll
        for (int hh = 0; hh < 2; hh++)
#pragma unroll
            for (int ct = 0; ct < 3; ct++)
                __builtin_amdgcn_raw_buffer_store_b128(ldv<u32x4>(scr + 3072 * hh + 1024 * ct + 16 * lane), rso, goff[hh] + 64 * ct, 0, STAUX);
    }
    STAMP(16);
}

extern "C" void kernel_launch(void* const* d_in, const int* in_sizes, int n_in, void* d_out, int out_size, void* d_ws, size_t ws_size,
                              hipStream_t stream) {
    convnext_kernel<<<256, 512, 0, stream>>>((const char*)d_in[0], (const char*)d_in[1], (const char*)d_in[2], (const char*)d_in[3], (char*)d_out
    );
}
```

```cpp
#define STAUX 16
#define PROBE 0
#define PROBE0 0
#define PCYC 0
#include <hip/hip_runtime.h>

typedef __attribute__((ext_vector_type(8))) __bf16 bf16x8;
typedef __attribute__((ext_vector_type(2))) __bf16 bf16x2;
typedef __attribute__((ext_vector_type(4))) short s16x4;
typedef __attribute__((ext_vector_type(16))) float f32x16;
typedef __attribute__((ext_vector_type(4))) float f32x4;
typedef __attribute__((ext_vector_type(2))) float f32x2;
typedef __attribute__((ext_vector_type(4))) unsigned u32x4;
typedef __attribute__((ext_vector_type(2))) unsigned u32x2;
typedef __attribute__((ext_vector_type(4))) int i32x4;
#define NST 18
#define AS3 __attribute__((address_space(3)))
#define DI __device__ __forceinline__
#define PIN(x) asm volatile("" :: "v"(x))
#define SB() __builtin_amdgcn_sched_barrier(0)

#define STAMP(i) do { if (i == 0 || i == PROBE || i == PROBE0) { SB(); unsigned long long t_, r_; asm volatile("s_memtime %0\n\ts_memrealtime %1\n\ts_waitcnt lgkmcnt(0)" : "=s"(t_), "=s"(r_) :: "memory"); TS[i] = (unsigned)t_; RT[i] = (unsigned)r_; SB(); } } while (0)

namespace {
constexpr int IMG = 224, XBYTES = IMG * IMG * 192;
constexpr int TBL_PS = 1128;
constexpr int TBL = 128;
constexpr int XP = 54272;
constexpr int SP = 147456;
constexpr int FS = 122880;
constexpr int CNT = 163840 - 64;
constexpr int H0 = CNT - 39264;
constexpr int W2B = 73728;
constexpr float GA = 0.46866869f;
constexpr float GC = 4.91222954f;
}

__shared__ __attribute__((aligned(16))) char smem[163840];

template <typename T> DI AS3 T* lp(unsigned off) { return (AS3 T*)((AS3 char*)smem + off); }
DI unsigned pk_bf16(float a, float b) { f32x2 v = {a, b}; return __builtin_bit_cast(unsigned, __builtin_convertvector(v, bf16x2)); }
DI f32x16 mfma32(u32x4 a, u32x4 b, f32x16 c) { return __builtin_amdgcn_mfma_f32_32x32x16_bf16(__builtin_bit_cast(bf16x8, a), __builtin_bit_cast(bf16x8, b), c, 0, 0, 0); }
DI f32x4 mfma16(u32x4 a, u32x4 b, f32x4 c) { return __builtin_amdgcn_mfma_f32_16x16x32_bf16(__builtin_bit_cast(bf16x8, a), __builtin_bit_cast(bf16x8, b), c, 0, 0, 0); }
DI u32x2 trread(unsigned off) { return __builtin_bit_cast(u32x2, __builtin_amdgcn_ds_read_tr16_b64_v4i16(lp<s16x4>(off))); }
DI u32x4 trfrag(unsigned off0, unsigned off1) { u32x2 a = trread(off0), b = trread(off1); return u32x4{a.x, a.y, b.x, b.y}; }
template <typename T> DI T ldv(unsigned off) { return *(volatile AS3 T*)lp<T>(off); }
template <typename T> DI void stv(unsigned off, T v) { *(volatile AS3 T*)lp<T>(off) = v; }
DI u32x4 rd2x64(unsigned off0, unsigned off1) { u32x2 a = ldv<u32x2>(off0), b = ldv<u32x2>(off1); return u32x4{a.x, a.y, b.x, b.y}; }
DI void lgkm0() { __builtin_amdgcn_s_waitcnt(0xC07F); }
DI void barrier() { asm volatile("s_waitcnt lgkmcnt(0)\n\ts_barrier" ::: "memory"); }
DI void dma(i32x4 rs, unsigned voff, unsigned soff, unsigned ldsdst) {
    asm volatile("s_mov_b32 m0, %3\n\ts_nop 0\n\tbuffer_load_dwordx4 %0, %1, %2 offen lds" :: "v"(voff), "s"(rs), "s"(soff), "s"(ldsdst) : "memory");
}
DI i32x4 mkrs(const void* p, int bytes) { i32x4 r; r.x = (int)(size_t)p; r.y = (int)((size_t)p >> 32) & 0xffff; r.z = bytes; r.w = 0x00020000; return r; }

struct Mlp {
    u32x4 h[6];
    f32x16 acc[3];
    u32x4 w1[6], w2[6];
    f32x16 X[3], Z[3];
    u32x4 G[2];
    unsigned b1, b2;
};

DI void load_w1(Mlp& m, unsigned a) {
#pragma unroll
    for (int s = 0; s < 6; s++) m.w1[s] = trfrag(a + 1024 * s, a + 1024 * s + 256);
}
DI void load_w2(Mlp& m, unsigned a) {
#pragma unroll
    for (int s = 0; s < 2; s++)
#pragma unroll
        for (int ct = 0; ct < 3; ct++) m.w2[s * 3 + ct] = trfrag(a + 3072 * s + 64 * ct, a + 3072 * s + 1536 + 64 * ct);
}

template <int K, bool G1, bool EXP, bool RCP, bool G2, bool L1, bool L2>
DI void step(Mlp& m, unsigned j1, unsigned j2) {
    constexpr int KE = (K + 2) % 3, KR = (K + 1) % 3;
    int te = 0;
    auto trans = [&](int n) {
#pragma unroll
        for (int q = 0; q < n; q++) {
            int t = te++;
            if (t < 16) { if (EXP) { float v = __builtin_amdgcn_exp2f(-m.Z[KE][t]); PIN(v); m.Z[KE][t] = v; } }
            else if (t < 32) { if (RCP) { float v = __builtin_amdgcn_rcpf(m.Z[KR][t - 16]); PIN(v); m.Z[KR][t - 16] = v; } }
        }
    };
    const f32x16 zero = {};
#pragma unroll
    for (int g = 0; g < 12; g++) {
        if (g < 6) { if (G1) m.X[K] = mfma32(m.w1[g], m.h[g], g == 0 ? zero : m.X[K]); }
        else if (G2) { int s = (g - 6) / 3, ct = (g - 6) % 3; m.acc[ct] = mfma32(m.w2[s * 3 + ct], m.G[s], m.acc[ct]); }
        SB();
        if (g < 4) {
            if (G2) {
                m.G[g >> 1][(g & 1) * 2] = pk_bf16(m.Z[K][4 * g], m.Z[K][4 * g + 1]);
                m.G[g >> 1][(g & 1) * 2 + 1] = pk_bf16(m.Z[K][4 * g + 2], m.Z[K][4 * g + 3]);
            }
            trans(2);
        } else trans(3);
        SB();
    }
    if (L1) load_w1(m, j1);
    if (L2) load_w2(m, j2);
    SB();
    const f32x2 ca = {GA, GA}, cc = {GC, GC};
#pragma unroll
    for (int q = 0; q < 8; q++) {
        if (EXP) { f32x2 e = {m.Z[KE][2 * q], m.Z[KE][2 * q + 1]}; e = __builtin_elementwise_fma(e, ca, ca); PIN(e); m.Z[KE][2 * q] = e.x; m.Z[KE][2 * q + 1] = e.y; }
    }
#pragma unroll
    for (int q = 0; q < 8; q++) {
        if (RCP) { f32x2 r = {m.Z[KR][2 * q], m.Z[KR][2 * q + 1]}, x = {m.X[KR][2 * q], m.X[KR][2 * q + 1]}; r = r * x; PIN(r); m.Z[KR][2 * q] = r.x; m.Z[KR][2 * q + 1] = r.y; }
    }
#pragma unroll
    for (int q = 0; q < 8; q++) {
        if (G1) { f32x2 x = {m.X[K][2 * q], m.X[K][2 * q + 1]}; f32x2 t = __builtin_elementwise_fma(x, x, cc); t = t * x; PIN(t); m.Z[K][2 * q] = t.x; m.Z[K][2 * q + 1] = t.y; }
    }
    lgkm0();
    SB();
}

DI void wait_cnt(unsigned off, unsigned target) {
    while (__builtin_amdgcn_readfirstlane(*(volatile AS3 unsigned*)lp<unsigned>(off)) < target) __builtin_amdgcn_s_sleep(1);
    asm volatile("" ::: "memory");
}
DI void signal_cnt(unsigned off, int lane) {
    asm volatile("s_waitcnt lgkmcnt(0)" ::: "memory");
    if (lane == 0) __hip_atomic_fetch_add(lp<unsigned>(off), 1u, __ATOMIC_RELAXED, __HIP_MEMORY_SCOPE_WORKGROUP);
    asm volatile("" ::: "memory");
}
DI unsigned haddr(int p) { return H0 + p * 200 + (p / 28) * 8; }

#define OPQ(x) asm volatile("" : "+v"(x))
DI i32x4 row_rs(const char* x, int gy) {
    const char* p = x + (long)gy * (IMG * 192);
    return mkrs(p, (unsigned)gy < (unsigned)IMG ? IMG * 192 : 0);
}
DI void xload_row(u32x4 (&d)[4], const unsigned (&c)[4], i32x4 rs) {
    asm volatile("buffer_load_dwordx4 %0, %4, %8, 0 offen\n\tbuffer_load_dwordx4 %1, %5, %8, 0 offen\n\t"
                 "buffer_load_dwordx4 %2, %6, %8, 0 offen\n\tbuffer_load_dwordx4 %3, %7, %8, 0 offen"
                 : "=&v"(d[0]), "=&v"(d[1]), "=&v"(d[2]), "=&v"(d[3]) : "v"(c[0]), "v"(c[1]), "v"(c[2]), "v"(c[3]), "s"(rs) : "memory");
}
DI void xload_row_hi(u32x4 (&d)[4], const unsigned (&c)[4], i32x4 rs, int w) {
    asm volatile("s_cmp_lt_u32 %9, 4\n\ts_cbranch_scc1 .Lskip%=\n\t"
                 "buffer_load_dwordx4 %0, %4, %8, 0 offen\n\tbuffer_load_dwordx4 %1, %5, %8, 0 offen\n\t"
                 "buffer_load_dwordx4 %2, %6, %8, 0 offen\n\tbuffer_load_dwordx4 %3, %7, %8, 0 offen\n.Lskip%=:"
                 : "=&v"(d[0]), "=&v"(d[1]), "=&v"(d[2]), "=&v"(d[3]) : "v"(c[0]), "v"(c[1]), "v"(c[2]), "v"(c[3]), "s"(rs), "s"(w) : "memory", "scc");
}
template <int N> DI void xwait(u32x4 (&d)[4]) { asm volatile("s_waitcnt vmcnt(%c4)" : "+v"(d[0]), "+v"(d[1]), "+v"(d[2]), "+v"(d[3]) : "i"(N) : "memory"); }
DI void xpose_row(const u32x4 (&v)[4], unsigned a) {
#pragma unroll
    for (int d = 0; d < 4; d++) {
        u32x2 lo = {__builtin_amdgcn_perm(v[1][d], v[0][d], 0x05040100u), __builtin_amdgcn_perm(v[3][d], v[2][d], 0x05040100u)};
        u32x2 hi = {__builtin_amdgcn_perm(v[1][d], v[0][d], 0x07060302u), __builtin_amdgcn_perm(v[3][d], v[2][d], 0x07060302u)};
        stv<u32x2>(a + (2 * d) * 960, lo);
        stv<u32x2>(a + (2 * d + 1) * 960, hi);
    }
}

__global__ __launch_bounds__(512) void convnext_kernel(const char* __restrict__ x, const char* __restrict__ dw, const char* __restrict__ w1,
                                                       const char* __restrict__ w2, char* __restrict__ out
) {
    unsigned TS[24], RT[24];
    STAMP(0);
    const int tid = threadIdx.x, lane = tid & 63, w = __builtin_amdgcn_readfirstlane(tid >> 6);
    const int b = blockIdx.x, xcd = b & 7, bi = b >> 3;
    const int ty = (xcd >> 1) * 4 + (bi >> 3), tx = (xcd & 1) * 8 + (bi & 7);
    const i32x4 rsx = mkrs(x, XBYTES), rsd = mkrs(dw, 49 * 192);

    unsigned D[7];
    const int ti = tid / 48, tcp = tid % 48;
    const unsigned doff = ti * 1344 + 4 * tcp;
    const int cgp = lane / 12, cg = lane % 12;
    unsigned coff[4];
#pragma unroll
    for (int i = 0; i < 4; i++) { coff[i] = (tx * 14 - 3 + 4 * cgp + i) * 192 + cg * 16; PIN(coff[i]); }
    PIN(doff);
    asm volatile("s_cmp_ge_u32 %9, 6\n\ts_cbranch_scc1 .Lskip%=\n\t"
                 "buffer_load_dword %0, %7, %8, 0 offen\n\tbuffer_load_dword %1, %7, %8, 0 offen offset:192\n\tbuffer_load_dword %2, %7, %8, 0 offen offset:384\n\t"
                 "buffer_load_dword %3, %7, %8, 0 offen offset:576\n\tbuffer_load_dword %4, %7, %8, 0 offen offset:768\n\tbuffer_load_dword %5, %7, %8, 0 offen offset:960\n\t"
                 "buffer_load_dword %6, %7, %8, 0 offen offset:1152\n.Lskip%=:"
                 : "=&v"(D[0]), "=&v"(D[1]), "=&v"(D[2]), "=&v"(D[3]), "=&v"(D[4]), "=&v"(D[5]), "=&v"(D[6]) : "v"(doff), "s"(rsd), "s"(w) : "memory", "scc");
    u32x4 xv0[4], xv1[4], xv2[4];
    const int gy0 = ty * 14 - 3 + w;
    xload_row(xv0, coff, row_rs(x, gy0));
    xload_row_hi(xv2, coff, row_rs(x, gy0 + 12), w);
    STAMP(1);
    if (tid < 480) {
        unsigned a = XP + 40 + 48 * tid + 16 * (tid / 160);
#pragma unroll
        for (int k = 0; k < 4; k++) stv<u32x2>(a + k * 23088, u32x2{0, 0});
    }
    const int g4 = lane >> 4, n16 = lane & 15;
    unsigned aoff;
    bool av[4][2];
    {
        int py = n16 >> 3, px = n16 & 7, ry = g4 >> 1, xh = g4 & 1;
        aoff = 6 * w * TBL_PS + (ry - py) * 80 + (8 * xh - px + 3) * 8 + TBL;
        OPQ(aoff);
#pragma unroll
        for (int s = 0; s < 4; s++)
#pragma unroll
            for (int q = 0; q < 2; q++) {
                int i = 2 * s + ry - py, u = 8 * xh - px + 4 * q;
                av[s][q] = i >= 0 && i <= 6 && u >= -3 && u <= 6;
            }
    }
    u32x2 Ah[12][4][2];
#pragma unroll
    for (int cl = 0; cl < 9; cl++)
#pragma unroll
        for (int s = 0; s < 4; s++)
#pragma unroll
            for (int q = 0; q < 2; q++) { Ah[cl][s][q] = u32x2{0, 0}; OPQ(Ah[cl][s][q]); }
    auto load_A = [&](int c0, int c1) {
#pragma unroll
        for (int q = 0; q < 2; q++)
#pragma unroll
            for (int sg = 0; sg < 3; sg++)
                if (av[sg == 2 ? 3 : sg][q]) {
#pragma unroll
                    for (int s = (sg == 2 ? 3 : sg); s <= (sg == 0 ? 0 : sg + 1); s++)
#pragma unroll
                        for (int cl = c0; cl < c1; cl++) Ah[cl][s][q] = ldv<u32x2>(aoff + (cl >> 1) * TBL_PS + (cl & 1) * 560 + 160 * s + 32 * q);
                }
    };
    unsigned xa = XP + cg * (8 * 960 + 16) + (w & 1) * 480 + (w >> 1) * 48 + cgp * 8;
    unsigned ta = TBL + tcp * TBL_PS + ti * 80;
    OPQ(xa); OPQ(ta);
    asm volatile("s_cmp_ge_u32 %7, 4\n\ts_cbranch_scc1 .Lw%=\n\ts_waitcnt vmcnt(4)\n.Lw%=:\n\ts_waitcnt vmcnt(8)"
                 : "+v"(D[0]), "+v"(D[1]), "+v"(D[2]), "+v"(D[3]), "+v"(D[4]), "+v"(D[5]), "+v"(D[6]) : "s"(w) : "memory", "scc");
    STAMP(2);
    if (tid < 336) {
#pragma unroll
        for (int hi = 0; hi < 2; hi++) {
            unsigned P[12];
            P[0] = 0; P[1] = 0; P[10] = 0; P[11] = 0;
            P[2] = hi ? (D[0] & 0xffff0000u) : (D[0] << 16);
#pragma unroll
            for (int v = 0; v < 6; v++) P[v + 3] = __builtin_amdgcn_perm(D[v + 1], D[v], hi ? 0x07060302u : 0x05040100u);
            P[9] = hi ? (D[6] >> 16) : (D[6] & 0xffffu);
#pragma unroll
            for (int u = 0; u < 10; u++) stv<u32x2>(ta + hi * 560 + 8 * u, u32x2{P[u], P[u + 2]});
        }
    }
    xload_row(xv1, coff, row_rs(x, gy0 + 8));
    STAMP(3);
    barrier();
    STAMP(4);
    load_A(0, 9);
    SB();
    STAMP(5);
    {
        asm volatile("s_cmp_ge_u32 %4, 4\n\ts_cbranch_scc1 .Lw%=\n\ts_waitcnt vmcnt(4)\n.Lw%=:\n\ts_waitcnt vmcnt(8)"
                     : "+v"(xv0[0]), "+v"(xv0[1]), "+v"(xv0[2]), "+v"(xv0[3]) : "s"(w) : "memory", "scc");
        xpose_row(xv0, xa);
        SB();
        xwait<4>(xv2);
        if (w >= 4) xpose_row(xv2, xa + 6 * 48);
        SB();
#pragma unroll
        for (int cl = 9; cl < 12; cl++)
#pragma unroll
            for (int s = 0; s < 4; s++)
#pragma unroll
                for (int q = 0; q < 2; q++) { Ah[cl][s][q] = u32x2{0, 0}; OPQ(Ah[cl][s][q]); }
        load_A(9, 12);
        SB();
        STAMP(6);
        xwait<0>(xv1);
        STAMP(7);
        xpose_row(xv1, xa + 4 * 48);
        SB();
    }
    const i32x4 rw1 = mkrs(w1, 96 * 768), rw2 = mkrs(w2, 384 * 192);
    const unsigned v1 = (lane >> 2) * 768 + (lane & 3) * 16;
    const bool inN0 = n16 < 4 || n16 >= 12;
    const int idx8 = n16 < 4 ? n16 : (n16 >= 12 ? n16 - 8 : n16 - 4);
    const int hb = idx8 < 7 ? idx8 : 6;
    const int wb = (inN0 == (hb != 1 && hb != 6)) ? 1 : 0;
    const unsigned bl = XP + (g4 >> 1) * 480 + hb * 48 + (wb + (g4 & 1)) * 16 + 12 * w * 960;
    const unsigned bA = bl + ((12 * w) >> 3) * 16, bB = bl + ((12 * w + 4) >> 3) * 16;
    PIN(v1); PIN(bA); PIN(bB);
    STAMP(8);
    barrier();
    STAMP(9);
    f32x4 o[12];
    float s1[4], s2[4];
    {
        u32x4 B[12][4];
        auto baddr = [&](int cl, int s) { return (cl < 4 ? bA : (cl < 8 ? bB : bA + 16)) + cl * 960 + 48 * s; };
#pragma unroll
        for (int s = 0; s < 4; s++) B[0][s] = ldv<u32x4>(baddr(0, s));
#pragma unroll
        for (int s = 0; s < 4; s++) B[1][s] = ldv<u32x4>(baddr(1, s));
        SB();
#pragma unroll
        for (int cl = 0; cl < 12; cl++) {
            if (cl < 11) __builtin_amdgcn_s_waitcnt(0xC47F); else __builtin_amdgcn_s_waitcnt(0xC07F);
            SB();
#pragma unroll
            for (int s = 0; s < 4; s++) {
                const u32x4 a = {Ah[cl][s][0].x, Ah[cl][s][0].y, Ah[cl][s][1].x, Ah[cl][s][1].y};
                if (s == 0) asm volatile("v_mfma_f32_16x16x32_bf16 %0, %1, %2, 0" : "=&v"(o[cl]) : "v"(a), "v"(B[cl][s]));
                else asm volatile("v_mfma_f32_16x16x32_bf16 %0, %1, %2, %0" : "+v"(o[cl]) : "v"(a), "v"(B[cl][s]));
                SB();
                if (cl + 2 < 12) B[cl + 2][s] = ldv<u32x4>(baddr(cl + 2, s));
                if (s == 3 && (cl & 1) == 0) dma(rw1, v1, 64 * w + 12288 * (cl >> 1), 6144 * w + 1024 * (cl >> 1));
                SB();
                if (cl == 3) asm volatile("v_add_f32 %0, %1, %2" : "=&v"(s1[s]) : "v"(o[0][s]), "v"(o[1][s]));
                if (cl > 3) asm volatile("v_add_f32 %0, %0, %1" : "+v"(s1[s]) : "v"(o[cl - 2][s]));
                SB();
            }
        }
        asm volatile("s_nop 7" : "+v"(o[11]));
        f32x2 qa = {o[0][0], o[0][1]}, qb = {o[0][2], o[0][3]};
        qa *= qa; qb *= qb;
#pragma unroll
        for (int cl = 1; cl < 12; cl++) {
            f32x2 a = {o[cl][0], o[cl][1]}, c = {o[cl][2], o[cl][3]};
            qa = __builtin_elementwise_fma(a, a, qa); qb = __builtin_elementwise_fma(c, c, qb);
        }
        s2[0] = qa.x; s2[1] = qa.y; s2[2] = qb.x; s2[3] = qb.y;
#pragma unroll
        for (int e = 0; e < 4; e++) s1[e] += o[10][e] + o[11][e];
    }
    stv<f32x4>(SP + w * 2048 + lane * 16, f32x4{s1[0], s1[1], s1[2], s1[3]});
    stv<f32x4>(SP + w * 2048 + 1024 + lane * 16, f32x4{s2[0], s2[1], s2[2], s2[3]});
    const int hf = lane >> 5;
    unsigned fdst;
    {
        int slot = 32 * w + (lane & 31);
        int sl = slot >> 2, e = slot & 3, sn = sl & 15, sg = sl >> 4;
        int si = sn < 4 ? sn : (sn >= 12 ? sn - 8 : sn - 4);
        int swb = ((sn < 4 || sn >= 12) == (si != 1 && si != 6)) ? 1 : 0;
        int col = 8 * swb + 4 * (sg & 1) + e;
        int p = (si < 7 && col < 14) ? (2 * si + (sg >> 1)) * 14 + col : 199;
        fdst = FS + hf * 800 + p * 4;
        PIN(fdst);
    }
    const int col0 = 8 * wb + 4 * (g4 & 1);
    const int p0 = (2 * hb + (g4 >> 1)) * 14 + col0;
    const unsigned hdst = H0 + p0 * 200 + hb * 8 + 24 * w;
    PIN(hdst);
    const i32x4 rws = w < 6 ? rw2 : rw1;
    const unsigned vs = w < 6 ? lane * 16 : v1;
    const unsigned sstep = w < 6 ? 1024 : 12288;
    auto dma_stage = [&](int stage, int n) {
        int j = w < 6 ? w + 6 * stage : w + 2 + 2 * stage;
        dma(rws, vs, (w < 6 ? 6144 : 64) * j + sstep * n, (w < 6 ? W2B : 0) + 6144 * j + 1024 * n);
    };
    STAMP(10);
    barrier();
    {
        unsigned a = SP + hf * 1024 + (32 * w + (lane & 31)) * 4;
        float t[8];
#pragma unroll
        for (int q = 0; q < 8; q++) t[q] = *lp<float>(a + 2048 * q);
        float T = ((t[0] + t[1]) + (t[2] + t[3])) + ((t[4] + t[5]) + (t[6] + t[7]));
        auto sw = __builtin_amdgcn_permlane32_swap(__float_as_uint(T), __float_as_uint(T), false, false);
        const unsigned swa = sw[0], swb2 = sw[1];
        float mean = __uint_as_float(swa) * (1.f / 96.f);
        float var = __builtin_fmaf(-mean, mean, __uint_as_float(swb2) * (1.f / 96.f));
        var = var > 0.f ? var : 0.f;
        float r = GA * __builtin_amdgcn_rsqf(var + 1e-5f);
        stv<float>(fdst, hf ? -mean * r : r);
    }
    barrier();
    STAMP(11);
    {
        f32x2 ra = ldv<f32x2>(FS + p0 * 4), rb = ldv<f32x2>(FS + p0 * 4 + 8);
        f32x2 ma = ldv<f32x2>(FS + 800 + p0 * 4), mb = ldv<f32x2>(FS + 800 + p0 * 4 + 8);
        if (tid < 16) stv<unsigned>(CNT + 4 * tid, 0);
        f32x2 na[12], nb[12];
#pragma unroll
        for (int cl = 0; cl < 12; cl++) {
            na[cl] = __builtin_elementwise_fma(f32x2{o[cl][0], o[cl][1]}, ra, ma);
            nb[cl] = __builtin_elementwise_fma(f32x2{o[cl][2], o[cl][3]}, rb, mb);
        }
        if (idx8 < 7) {
#pragma unroll
            for (int q = 0; q < 3; q++) {
                stv<u32x2>(hdst + 8 * q, u32x2{pk_bf16(na[4 * q].x, na[4 * q + 1].x), pk_bf16(na[4 * q + 2].x, na[4 * q + 3].x)});
                stv<u32x2>(hdst + 200 + 8 * q, u32x2{pk_bf16(na[4 * q].y, na[4 * q + 1].y), pk_bf16(na[4 * q + 2].y, na[4 * q + 3].y)});
            }
            if (col0 < 12) {
#pragma unroll
                for (int q = 0; q < 3; q++) {
                    stv<u32x2>(hdst + 400 + 8 * q, u32x2{pk_bf16(nb[4 * q].x, nb[4 * q + 1].x), pk_bf16(nb[4 * q + 2].x, nb[4 * q + 3].x)});
                    stv<u32x2>(hdst + 600 + 8 * q, u32x2{pk_bf16(nb[4 * q].y, nb[4 * q + 1].y), pk_bf16(nb[4 * q + 2].y, nb[4 * q + 3].y)});
                }
            }
        }
    }
    Mlp m;
    {
        int q = (lane >> 2) & 3, p = lane & 3, gm = (lane >> 4) & 1;
        m.b1 = 512 * hf + 64 * q + 32 * gm + 8 * p;
        m.b2 = W2B + (4 * hf + q) * 192 + 32 * (p & 1) + 16 * gm + 8 * (p >> 1);
    }
    const int tile = w < 7 ? w : 4;
    auto hrow = [&](int t) { int p = t * 32 + (lane & 31); p = p < 196 ? p : 195; return haddr(p); };
    const unsigned hsrc = hrow(tile) + 16 * hf;
    PIN(hsrc); PIN(m.b1); PIN(m.b2);
    asm volatile("s_waitcnt vmcnt(0)" ::: "memory");
    barrier();
    STAMP(12);
    auto load_h = [&](unsigned a) {
#pragma unroll
        for (int s = 0; s < 6; s++) m.h[s] = rd2x64(a + 32 * s, a + 32 * s + 8);
    };
    const int skip = (w >= 4 && w < 7) ? w - 4 : 99;
    const int NG = w < 4 ? 4 : 3;
    auto grp = [&](int g) { return 18432 * (g + (g >= skip ? 1 : 0)); };
    load_h(hsrc);
    load_w1(m, m.b1 + grp(0));
    {
        const u32x4 z4 = {}; const f32x16 zero = {};
#pragma unroll
        for (int ct = 0; ct < 3; ct++) m.acc[ct] = mfma32(z4, z4, zero);
    }
    {
        unsigned a1 = m.b1 + grp(0);
        dma_stage(0, 0); dma_stage(0, 1); dma_stage(0, 2);
        step<0, true, false, false, false, true, false>(m, a1 + 6144, 0);
        dma_stage(0, 3); dma_stage(0, 4); dma_stage(0, 5);
        step<1, true, true, false, false, true, false>(m, a1 + 12288, 0);
        asm volatile("s_waitcnt vmcnt(0)" ::: "memory");
        barrier();
        STAMP(13);
        dma_stage(1, 0); dma_stage(1, 1); dma_stage(1, 2);
        step<2, true, true, true, false, true, true>(m, m.b1 + grp(1), m.b2 + grp(0));
        dma_stage(1, 3); dma_stage(1, 4); dma_stage(1, 5);
        if (w == 7) load_h(hrow(5) + 16 * hf);
    }
    STAMP(14);
    auto flush_partial = [&](int t) {
        if (t * 32 + (lane & 31) < 196) {
            unsigned a = hrow(t) + 32 * hf;
#pragma unroll
            for (int ct = 0; ct < 3; ct++)
#pragma unroll
                for (int q = 0; q < 4; q++)
                    stv<u32x2>(a + 64 * ct + 8 * q, u32x2{pk_bf16(m.acc[ct][4 * q], m.acc[ct][4 * q + 1]), pk_bf16(m.acc[ct][4 * q + 2], m.acc[ct][4 * q + 3])});
        }
        signal_cnt(CNT + 8 + 4 * (t - 4), lane);
    };
#pragma unroll 1
    for (int g = 1; g < NG; g++) {
        int j = grp(g), jn = g + 1 < NG ? grp(g + 1) : 0;
        unsigned a1 = m.b1 + j, a2 = m.b2 + grp(g - 1);
        step<0, true, true, true, true, true, true>(m, a1 + 6144, a2 + 6144);
        if (g == 1) { asm volatile("s_waitcnt vmcnt(0)" ::: "memory"); signal_cnt(CNT, lane); }
        step<1, true, true, true, true, true, true>(m, a1 + 12288, a2 + 12288);
        if (g == (skip < 99 ? 1 : 2)) wait_cnt(CNT, 8);
        step<2, true, true, true, true, true, true>(m, m.b1 + jn, m.b2 + j);
        if (g == 1) signal_cnt(CNT + 4, lane);
        if (w == 7) {
            if (g == 1) load_h(hrow(6) + 16 * hf);
            flush_partial(3 + g);
            const u32x4 z4 = {}; const f32x16 zero = {};
#pragma unroll
            for (int ct = 0; ct < 3; ct++) m.acc[ct] = mfma32(z4, z4, zero);
        }
    }
    const unsigned scr = w < 6 ? 6144 * w : SP;
    unsigned goff[2];
    if (w < 7) {
        wait_cnt(CNT + 4, 8);
#pragma unroll
        for (int hh = 0; hh < 2; hh++) {
            int px = (lane >> 2) + 16 * hh, p = 32 * w + px;
            int swz = ((px >> 1) & 3) ^ ((px >> 3) & 1);
            goff[hh] = p < 196 ? ((ty * 14 + p / 14) * IMG + tx * 14 + p % 14) * 192 + 16 * ((lane & 3) ^ swz) : 0x80000000u;
            if (p < 196) {
#pragma unroll
                for (int ct = 0; ct < 3; ct++) dma(rsx, goff[hh], 64 * ct, scr + 3072 * hh + 1024 * ct);
            }
        }
    }
    {
        unsigned a2 = m.b2 + grp(NG - 1);
        step<0, false, true, true, true, false, true>(m, 0, a2 + 6144);
        step<1, false, false, true, true, false, true>(m, 0, a2 + 12288);
        step<2, false, false, false, true, false, false>(m, 0, 0);
    }
    STAMP(15);
    if (w == 7) {
        flush_partial(6);
    } else {
        u32x4 id[2];
        {
            int row = lane & 31, r = (row & 3) + 4 * (row >> 3);
            bool mine = ((row >> 2) & 1) == hf;
#pragma unroll
            for (int u = 0; u < 2; u++)
#pragma unroll
                for (int d = 0; d < 4; d++) id[u][d] = (mine && (r >> 1) == 4 * u + d) ? ((r & 1) ? 0x3f800000u : 0x00003f80u) : 0u;
        }
        const int px = lane & 31, swz = ((px >> 1) & 3) ^ ((px >> 3) & 1);
        const unsigned sa = scr + 3072 * (px >> 4) + 64 * (px & 15);
        asm volatile("s_waitcnt vmcnt(0)" ::: "memory");
#pragma unroll
        for (int u = 0; u < 2; u++)
#pragma unroll
            for (int ct = 0; ct < 3; ct++) m.acc[ct] = mfma32(id[u], ldv<u32x4>(sa + 16 * ((2 * hf + u) ^ swz) + 1024 * ct), m.acc[ct]);
        if (w >= 4) {
            wait_cnt(CNT + 8 + 4 * (w - 4), 1);
            unsigned a = hrow(w) + 32 * hf;
#pragma unroll
            for (int u = 0; u < 2; u++)
#pragma unroll
                for (int ct = 0; ct < 3; ct++) m.acc[ct] = mfma32(id[u], rd2x64(a + 64 * ct + 16 * u, a + 64 * ct + 16 * u + 8), m.acc[ct]);
        }
        if (32 * w + px < 196) {
#pragma unroll
            for (int u = 0; u < 2; u++)
#pragma unroll
                for (int ct = 0; ct < 3; ct++)
                    stv<u32x4>(sa + 16 * ((2 * hf + u) ^ swz) + 1024 * ct,
                               u32x4{pk_bf16(m.acc[ct][8 * u], m.acc[ct][8 * u + 1]), pk_bf16(m.acc[ct][8 * u + 2], m.acc[ct][8 * u + 3]),
                                     pk_bf16(m.acc[ct][8 * u + 4], m.acc[ct][8 * u + 5]), pk_bf16(m.acc[ct][8 * u + 6], m.acc[ct][8 * u + 7])});
        }
        auto rso = __builtin_amdgcn_make_buffer_rsrc((void*)out, 0, XBYTES, 0x00020000);
#pragma unroll
        for (int hh = 0; hh < 2; hh++)
#pragma unroll
            for (int ct = 0; ct < 3; ct++)
                __builtin_amdgcn_raw_buffer_store_b128(ldv<u32x4>(scr + 3072 * hh + 1024 * ct + 16 * lane), rso, goff[hh] + 64 * ct, 0, STAUX);
    }
    STAMP(16);
    {
        asm volatile("s_waitcnt vmcnt(0)" ::: "memory");
        STAMP(17);
        unsigned d = PCYC ? (TS[PROBE] - TS[PROBE0]) / 24 : RT[PROBE] - RT[PROBE0];
        unsigned tgt = 5000 + 10 * d;
        while ((unsigned)__builtin_amdgcn_s_memrealtime() - RT[0] < tgt) __builtin_amdgcn_s_sleep(4);
    }
}

extern "C" void kernel_launch(void* const* d_in, const int* in_sizes, int n_in, void* d_out, int out_size, void* d_ws, size_t ws_size,
                              hipStream_t stream) {
    convnext_kernel<<<256, 512, 0, stream>>>((const char*)d_in[0], (const char*)d_in[1], (const char*)d_in[2], (const char*)d_in[3], (char*)d_out
    );
}
```

```cpp
#include <hip/hip_runtime.h>

__shared__ __attribute__((aligned(16))) char smem[163840];

__global__ __launch_bounds__(512) void convnext_kernel(const char* x, const char* dw, const char* w1, const char* w2, char* out) {
    asm volatile(
        "s_load_dwordx8 s[8:15], %0, 0x0\n"
        "s_load_dwordx2 s[16:17], %0, 0x20\n"
        "s_getpc_b64 s[98:99]\n"
        "v_readfirstlane_b32 s20, v0\n"
        "v_and_b32 v240, 63, v0\n"
        "v_mul_u32_u24 v242, 5042, v240\n"
        "s_and_b32 s23, %1, 1\n"
        "s_lshl_b32 s23, s23, 3\n"
        "s_bfe_u32 s22, %1, 0x30003\n"
        "v_lshrrev_b32 v12, 16, v242\n"
        "v_mul_u32_u24 v241, 43, v240\n"
        "v_lshlrev_b32 v2, 4, v240\n"
        "s_or_b32 s22, s22, s23\n"
        "s_mul_i32 s22, s22, 14\n"
        "s_lshl_b32 s23, %1, 1\n"
        "s_and_b32 s23, s23, 12\n"
        "v_sub_u32 v12, v240, v12\n"
        "v_lshrrev_b32_e64 v241, 9, v241\n"
        "s_lshr_b32 s21, %1, 6\n"
        "s_add_u32 s21, s21, s23\n"
        "s_mul_i32 s21, s21, 14\n"
        "s_lshr_b32 s20, s20, 6\n"
        "v_mul_u32_u24 v6, 576, v241\n"
        "s_mul_i32 s37, s20, 3336\n"
        "s_add_u32 m0, s37, 8888\n"
        "s_mul_i32 s36, s20, 1344\n"
        "s_mul_i32 s23, s22, 192\n"
        "s_sub_u32 s23, s23, 576\n"
        "v_lshlrev_b32_e64 v12, 4, v12\n"
        "v_add3_u32 v6, v6, v2, s23\n"
        "s_add_u32 s44, s21, s20\n"
        "s_sub_u32 s44, s44, 3\n"
        "v_add_u32 v242, 322688, v242\n"
        "v_add_u32 v13, 192, v6\n"
        "v_lshrrev_b32_e64 v242, 16, v242\n"
        "v_add_u32 v14, 384, v6\n"
        "v_sub_u32_e64 v242, v240, v242\n"
        "v_add_u32 v15, 576, v6\n"
        "v_add_u32 v242, 64, v242\n"
        "s_cmp_lt_u32 s20, 7\n"
        "s_cselect_b32 s30, 1344, 0\n"
        "s_mov_b32 s31, 0x20000\n"
        "s_mov_b32 s27, 0x20000\n"
        "v_lshlrev_b32_e64 v242, 4, v242\n"
        "s_lshl_b32 s39, s37, 1\n"
        "s_add_u32 m0, s39, 8888\n"
        "s_lshl_b32 s36, s36, 1\n"
        "s_waitcnt lgkmcnt(0)\n"
        "s_cmp_lt_u32 s20, 4\n"
        "s_cbranch_scc0 .L1_%=\n"
        "s_add_u32 s28, s10, s36\n"
        "s_addc_u32 s29, s11, 0\n"
        "s_and_b32 s29, s29, 0xffff\n"
        "buffer_load_dwordx4 v12, s[28:31], 0 offen lds\n"
        "s_add_u32 m0, s39, 9912\n"
        "s_nop 0\n"
        "buffer_load_dwordx4 v242, s[28:31], 0 offen lds\n"
        "s_add_u32 s28, s28, 1344\n"
        "s_addc_u32 s29, s29, 0\n"
        "s_add_u32 m0, s39, 12224\n"
        "s_cmp_lt_u32 s20, 3\n"
        "s_cselect_b32 s30, 1344, 0\n"
        "buffer_load_dwordx4 v12, s[28:31], 0 offen lds\n"
        "s_add_u32 m0, s39, 13248\n"
        "s_nop 0\n"
        "buffer_load_dwordx4 v242, s[28:31], 0 offen lds\n"
        ".L1_%=:\n"
        "s_and_b32 s23, %1, 56\n"
        "s_add_u32 s23, s23, s20\n"
        "s_mul_i32 s23, s23, 336\n"
        "v_lshl_add_u32 v243, v2, 3, s23\n"
        "s_add_u32 s24, s98, 1024\n"
        "s_addc_u32 s25, s99, 0\n"
        "s_and_b32 s25, s25, 0xffff\n"
        "s_mov_b32 s26, 21504\n"
        "s_mov_b32 m0, 70656\n"
        "s_mov_b64 exec, 7\n"
        "buffer_load_dword v243, s[24:27], 0 offen lds\n"
        "s_mov_b64 exec, -1\n"
        "s_add_u32 s45, s44, 0\n"
        "s_mul_i32 s46, s45, 43008\n"
        "s_ashr_i32 s23, s46, 31\n"
        "s_add_u32 s24, s8, s46\n"
        "s_addc_u32 s25, s9, s23\n"
        "s_and_b32 s25, s25, 0xffff\n"
        "s_cmp_lt_u32 s45, 224\n"
        "s_cselect_b32 s26, 43008, 0\n"
        "buffer_load_dwordx4 v[16:19], v6, s[24:27], 0 offen\n"
        "buffer_load_dwordx4 v[20:23], v13, s[24:27], 0 offen\n"
        "buffer_load_dwordx4 v[24:27], v14, s[24:27], 0 offen\n"
        "buffer_load_dwordx4 v[28:31], v15, s[24:27], 0 offen\n"
        "v_mov_b64 v[252:253], 0\n"
        "v_lshrrev_b32 v243, 1, v2\n"
        "v_add_u32 v244, s37, v243\n"
        "v_lshlrev_b32 v245, 3, v0\n"
        "ds_write_b64 v244, v[252:253] offset:10936\n"
        "ds_write_b64 v244, v[252:253] offset:11448\n"
        "ds_write_b64 v244, v[252:253] offset:11712\n"
        "ds_write_b64 v245, v[252:253] offset:4096\n"
        "ds_write_b64 v245, v[252:253] offset:4792\n"
        "v_and_b32_e64 v243, 24, v243\n"
        "v_add_u32 v243, 163776, v243\n"
        "ds_write_b64 v243, v[252:253]\n"
        "s_mul_i32 s57, s20, 24\n"
        "v_mul_u32_u24 v243, 0x19a, v0\n"
        "v_mul_u32_u24 v244, 48, v0\n"
        "v_lshrrev_b32 v243, 12, v243\n"
        "v_and_b32 v243, 48, v243\n"
        "s_mov_b32 s23, 71464\n"
        "v_add3_u32 v243, v243, v244, s23\n"
        "v_add_u32 v244, 46176, v243\n"
        "ds_write_b64 v243, v[252:253]\n"
        "ds_write_b64 v243, v[252:253] offset:23088\n"
        "ds_write_b64 v244, v[252:253]\n"
        "s_cmp_eq_u32 s20, 7\n"
        "s_cselect_b32 exec_hi, 0, -1\n"
        "ds_write_b64 v244, v[252:253] offset:23088\n"
        "s_mov_b32 exec_hi, -1\n"
        "v_lshrrev_b32 v247, 5, v240\n"
        "v_bfe_u32 v243, v240, 3, 1\n"
        "v_lshlrev_b32 v244, 0, v247\n"
        "v_sub_u32 v243, v244, v243\n"
        "v_bfe_u32 v248, v240, 4, 1\n"
        "v_and_b32 v245, 7, v240\n"
        "v_lshlrev_b32 v244, 3, v248\n"
        "v_sub_u32_e64 v245, v244, v245\n"
        "v_mul_i32_i24 v243, 3336, v243\n"
        "v_mul_i32_i24 v3, 208, v245\n"
        "s_add_u32 s23, s57, 8888\n"
        "v_add3_u32 v3, v3, v243, s23\n"
        "v_mul_u32_u24 v4, 7696, v240\n"
        "s_mov_b32 s23, -92344\n"
        "v_mad_i32_i24 v4, v241, s23, v4\n"
        "s_and_b32 s23, s20, 1\n"
        "s_mul_i32 s23, s23, 456\n"
        "s_add_u32 s23, s23, s57\n"
        "s_add_u32 s23, s23, 71424\n"
        "v_add_u32_e64 v4, s23, v4\n"
        "s_mov_b32 s47, 0x05040100\n"
        "s_mov_b32 s48, 0x07060302\n"
        "v_lshlrev_b32 v243, 2, v240\n"
        "v_and_b32 v243, 60, v243\n"
        "s_mov_b32 s98, 0x64307521\n"
        "s_mov_b32 s99, 0xfcb9eda8\n"
        "v_lshrrev_b64 v[244:245], v243, s[98:99]\n"
        "v_bfe_u32 v243, v244, 1, 3\n"
        "v_and_b32_e64 v245, 1, v244\n"
        "v_mul_u32_u24 v5, 480, v247\n"
        "v_mad_u32_u24 v5, v243, 48, v5\n"
        "v_add_u32_e64 v244, v245, v248\n"
        "v_lshl_add_u32 v5, v244, 4, v5\n"
        "s_mul_i32 s23, s20, 11544\n"
        "s_add_u32 s23, s23, 71424\n"
        "s_and_b32 s23, s23, -16\n"
        "v_add_u32 v5, s23, v5\n"
        "v_mul_u32_u24 v243, 5608, v243\n"
        "v_mul_u32_u24 v245, 1600, v245\n"
        "v_add_u32_e64 v1, v243, v245\n"
        "v_bfe_u32 v243, v240, 2, 2\n"
        "v_lshl_add_u32 v243, v247, 2, v243\n"
        "v_mul_u32_u24 v9, 192, v243\n"
        "v_and_b32_e64 v7, 48, v2\n"
        "v_lshl_add_u32 v7, v9, 2, v7\n"
        "v_lshl_add_u32 v7, v248, 6, v7\n"
        "s_bitcmp1_b32 s20, 0\n"
        "s_cselect_b32 s55, 2, 1\n"
        "s_cselect_b32 s49, 18432, 0\n"
        "s_add_u32 s54, s20, 4\n"
        "s_lshr_b32 s54, s54, 1\n"
        "s_mov_b32 s51, 36864\n"
        "s_cmp_ge_u32 s20, 6\n"
        "s_cselect_b32 s45, s51, 0\n"
        "s_cselect_b32 s51, -36864, s51\n"
        "s_add_u32 s49, s49, s45\n"
        "s_cmp_lt_u32 s20, 4\n"
        "s_cselect_b32 s55, 0, s55\n"
        "s_cselect_b32 s49, 0, s49\n"
        "s_cselect_b32 s54, s20, s54\n"
        "s_mov_b32 s56, 0x3eeff558\n"
        "s_mov_b32 s58, 0x409d30fc\n"
        "v_and_b32 v12, 31, v0\n"
        "v_lshlrev_b32 v11, 2, v12\n"
        "s_lshl_b32 s45, s54, 5\n"
        "v_add_u32_e64 v12, s45, v12\n"
        "v_mul_u32_u24 v10, 2341, v12\n"
        "v_lshrrev_b32 v10, 16, v10\n"
        "v_mul_u32_u24 v12, 25, v12\n"
        "v_add_lshl_u32 v10, v10, v12, 3\n"
        "v_add_u32 v10, 124512, v10\n"
        "v_and_b32 v8, 12, v0\n"
        "v_lshlrev_b32 v8, 4, v8\n"
        "v_bfe_u32 v12, v0, 5, 1\n"
        "v_lshl_add_u32 v10, v12, 4, v10\n"
        "v_lshl_add_u32 v8, v12, 10, v8\n"
        "v_lshl_add_u32 v11, v12, 10, v11\n"
        "v_mul_u32_u24 v12, 2800, v12\n"
        "s_add_u32 s45, s57, 124512\n"
        "v_add3_u32 v1, v1, v12, s45\n"
        "s_lshl_b32 s45, s20, 7\n"
        "s_add_u32 s45, s45, 71424\n"
        "v_add_u32 v11, s45, v11\n"
        "v_and_b32_e64 v12, 16, v0\n"
        "v_lshl_add_u32 v8, v12, 1, v8\n"
        "v_add_u32_e64 v9, v9, v12\n"
        "v_mad_u32_u24 v1, v12, 50, v1\n"
        "v_and_b32_e64 v12, 3, v0\n"
        "v_lshl_add_u32 v8, v12, 3, v8\n"
        "v_and_b32_e64 v12, 1, v0\n"
        "v_lshl_add_u32 v9, v12, 5, v9\n"
        "v_and_b32_e64 v12, 2, v0\n"
        "v_lshl_add_u32 v9, v12, 2, v9\n"
        "v_add_u32 v9, 73728, v9\n"
        "s_add_u32 s45, s44, 8\n"
        "s_mul_i32 s46, s45, 43008\n"
        "s_ashr_i32 s23, s46, 31\n"
        "s_add_u32 s24, s8, s46\n"
        "s_addc_u32 s25, s9, s23\n"
        "s_and_b32 s25, s25, 0xffff\n"
        "s_cmp_lt_u32 s45, 224\n"
        "s_cselect_b32 s26, 43008, 0\n"
        "s_cmp_lt_u32 s20, 4\n"
        "s_cbranch_scc0 .L2_%=\n"
        "buffer_load_dwordx4 v[32:35], v6, s[24:27], 0 offen\n"
        "buffer_load_dwordx4 v[36:39], v13, s[24:27], 0 offen\n"
        "buffer_load_dwordx4 v[40:43], v14, s[24:27], 0 offen\n"
        "buffer_load_dwordx4 v[44:47], v15, s[24:27], 0 offen\n"
        "s_waitcnt vmcnt(9)\n"
        ".L2_%=:\n"
        "s_waitcnt lgkmcnt(0)\n"
        "s_barrier\n"
        "s_cmp_lt_u32 s20, 4\n"
        "s_cbranch_scc0 .L3_%=\n"
        "s_add_u32 s45, s44, 16\n"
        "s_mul_i32 s46, s45, 43008\n"
        "s_ashr_i32 s23, s46, 31\n"
        "s_add_u32 s24, s8, s46\n"
        "s_nop 0\n"
        "s_addc_u32 s25, s9, s23\n"
        "s_and_b32 s25, s25, 0xffff\n"
        "s_cmp_lt_u32 s45, 224\n"
        "s_cselect_b32 s26, 43008, 0\n"
        "ds_read_b64 v[224:225], v3 offset:0\n"
        "ds_read_b64 v[226:227], v3 offset:208\n"
        "ds_read_b64 v[228:229], v3 offset:416\n"
        "ds_read_b64 v[230:231], v3 offset:624\n"
        "ds_read_b64 v[232:233], v3 offset:832\n"
        "ds_read_b64 v[234:235], v3 offset:1040\n"
        "ds_read_b64 v[236:237], v3 offset:1248\n"
        "ds_read_b64 v[238:239], v3 offset:1456\n"
        "ds_read_b64 v[208:209], v3 offset:6672\n"
        "ds_read_b64 v[210:211], v3 offset:6880\n"
        "ds_read_b64 v[212:213], v3 offset:7088\n"
        "ds_read_b64 v[214:215], v3 offset:7296\n"
        "ds_read_b64 v[216:217], v3 offset:7504\n"
        "ds_read_b64 v[218:219], v3 offset:7712\n"
        "ds_read_b64 v[220:221], v3 offset:7920\n"
        "ds_read_b64 v[222:223], v3 offset:8128\n"
        "buffer_load_dwordx4 v[48:51], v6, s[24:27], 0 offen\n"
        "s_nop 0\n"
        "s_waitcnt lgkmcnt(8)\n"
        "v_perm_b32 v240, v226, v224, s47\n"
        "v_perm_b32 v244, v226, v224, s48\n"
        "v_perm_b32 v241, v230, v228, s47\n"
        "v_perm_b32 v245, v230, v228, s48\n"
        "v_perm_b32 v242, v234, v232, s47\n"
        "v_perm_b32 v246, v234, v232, s48\n"
        "v_perm_b32 v243, v238, v236, s47\n"
        "v_perm_b32 v247, v238, v236, s48\n"
        "v_perm_b32 v248, v227, v225, s47\n"
        "v_perm_b32 v252, v227, v225, s48\n"
        "v_perm_b32 v249, v231, v229, s47\n"
        "v_perm_b32 v253, v231, v229, s48\n"
        "v_perm_b32 v250, v235, v233, s47\n"
        "v_perm_b32 v254, v235, v233, s48\n"
        "v_perm_b32 v251, v239, v237, s47\n"
        "v_perm_b32 v255, v239, v237, s48\n"
        "ds_read_b64 v[192:193], v3 offset:13344\n"
        "ds_read_b64 v[194:195], v3 offset:13552\n"
        "ds_read_b64 v[196:197], v3 offset:13760\n"
        "ds_read_b64 v[198:199], v3 offset:13968\n"
        "ds_read_b64 v[200:201], v3 offset:14176\n"
        "ds_read_b64 v[202:203], v3 offset:14384\n"
        "ds_read_b64 v[204:205], v3 offset:14592\n"
        "ds_read_b64 v[206:207], v3 offset:14800\n"
        "buffer_load_dwordx4 v[52:55], v13, s[24:27], 0 offen\n"
        "s_nop 0\n"
        "s_waitcnt lgkmcnt(8)\n"
        "v_perm_b32 v224, v210, v208, s47\n"
        "v_perm_b32 v228, v210, v208, s48\n"
        "v_perm_b32 v225, v214, v212, s47\n"
        "v_perm_b32 v229, v214, v212, s48\n"
        "v_perm_b32 v226, v218, v216, s47\n"
        "v_perm_b32 v230, v218, v216, s48\n"
        "v_perm_b32 v227, v222, v220, s47\n"
        "v_perm_b32 v231, v222, v220, s48\n"
        "v_perm_b32 v232, v211, v209, s47\n"
        "v_perm_b32 v236, v211, v209, s48\n"
        "v_perm_b32 v233, v215, v213, s47\n"
        "v_perm_b32 v237, v215, v213, s48\n"
        "v_perm_b32 v234, v219, v217, s47\n"
        "v_perm_b32 v238, v219, v217, s48\n"
        "v_perm_b32 v235, v223, v221, s47\n"
        "v_perm_b32 v239, v223, v221, s48\n"
        "ds_read_b64 v[176:177], v3 offset:20016\n"
        "ds_read_b64 v[178:179], v3 offset:20224\n"
        "ds_read_b64 v[180:181], v3 offset:20432\n"
        "ds_read_b64 v[182:183], v3 offset:20640\n"
        "ds_read_b64 v[184:185], v3 offset:20848\n"
        "ds_read_b64 v[186:187], v3 offset:21056\n"
        "ds_read_b64 v[188:189], v3 offset:21264\n"
        "ds_read_b64 v[190:191], v3 offset:21472\n"
        "buffer_load_dwordx4 v[56:59], v14, s[24:27], 0 offen\n"
        "s_nop 0\n"
        "s_waitcnt lgkmcnt(8)\n"
        "v_perm_b32 v208, v194, v192, s47\n"
        "v_perm_b32 v212, v194, v192, s48\n"
        "v_perm_b32 v209, v198, v196, s47\n"
        "v_perm_b32 v213, v198, v196, s48\n"
        "v_perm_b32 v210, v202, v200, s47\n"
        "v_perm_b32 v214, v202, v200, s48\n"
        "v_perm_b32 v211, v206, v204, s47\n"
        "v_perm_b32 v215, v206, v204, s48\n"
        "v_perm_b32 v216, v195, v193, s47\n"
        "v_perm_b32 v220, v195, v193, s48\n"
        "v_perm_b32 v217, v199, v197, s47\n"
        "v_perm_b32 v221, v199, v197, s48\n"
        "v_perm_b32 v218, v203, v201, s47\n"
        "v_perm_b32 v222, v203, v201, s48\n"
        "v_perm_b32 v219, v207, v205, s47\n"
        "v_perm_b32 v223, v207, v205, s48\n"
        "ds_read_b64 v[160:161], v3 offset:8\n"
        "ds_read_b64 v[162:163], v3 offset:216\n"
        "ds_read_b64 v[164:165], v3 offset:424\n"
        "ds_read_b64 v[166:167], v3 offset:632\n"
        "ds_read_b64 v[168:169], v3 offset:840\n"
        "ds_read_b64 v[170:171], v3 offset:1048\n"
        "ds_read_b64 v[172:173], v3 offset:1256\n"
        "ds_read_b64 v[174:175], v3 offset:1464\n"
        "buffer_load_dwordx4 v[60:63], v15, s[24:27], 0 offen\n"
        "s_nop 0\n"
        "s_waitcnt lgkmcnt(8)\n"
        "v_perm_b32 v192, v178, v176, s47\n"
        "v_perm_b32 v196, v178, v176, s48\n"
        "v_perm_b32 v193, v182, v180, s47\n"
        "v_perm_b32 v197, v182, v180, s48\n"
        "v_perm_b32 v194, v186, v184, s47\n"
        "v_perm_b32 v198, v186, v184, s48\n"
        "v_perm_b32 v195, v190, v188, s47\n"
        "v_perm_b32 v199, v190, v188, s48\n"
        "v_perm_b32 v200, v179, v177, s47\n"
        "v_perm_b32 v204, v179, v177, s48\n"
        "v_perm_b32 v201, v183, v181, s47\n"
        "v_perm_b32 v205, v183, v181, s48\n"
        "v_perm_b32 v202, v187, v185, s47\n"
        "v_perm_b32 v206, v187, v185, s48\n"
        "v_perm_b32 v203, v191, v189, s47\n"
        "v_perm_b32 v207, v191, v189, s48\n"
        "ds_read_b64 v[144:145], v3 offset:6680\n"
        "ds_read_b64 v[146:147], v3 offset:6888\n"
        "ds_read_b64 v[148:149], v3 offset:7096\n"
        "ds_read_b64 v[150:151], v3 offset:7304\n"
        "ds_read_b64 v[152:153], v3 offset:7512\n"
        "ds_read_b64 v[154:155], v3 offset:7720\n"
        "ds_read_b64 v[156:157], v3 offset:7928\n"
        "ds_read_b64 v[158:159], v3 offset:8136\n"
        "s_lshl_b32 s23, s20, 4\n"
        "s_and_b32 s23, s23, 16\n"
        "v_add_u32 v6, s23, v5\n"
        "s_waitcnt lgkmcnt(8)\n"
        "v_perm_b32 v176, v162, v160, s47\n"
        "v_perm_b32 v180, v162, v160, s48\n"
        "v_perm_b32 v177, v166, v164, s47\n"
        "v_perm_b32 v181, v166, v164, s48\n"
        "v_perm_b32 v178, v170, v168, s47\n"
        "v_perm_b32 v182, v170, v168, s48\n"
        "v_perm_b32 v179, v174, v172, s47\n"
        "v_perm_b32 v183, v174, v172, s48\n"
        "v_perm_b32 v184, v163, v161, s47\n"
        "v_perm_b32 v188, v163, v161, s48\n"
        "v_perm_b32 v185, v167, v165, s47\n"
        "v_perm_b32 v189, v167, v165, s48\n"
        "v_perm_b32 v186, v171, v169, s47\n"
        "v_perm_b32 v190, v171, v169, s48\n"
        "v_perm_b32 v187, v175, v173, s47\n"
        "v_perm_b32 v191, v175, v173, s48\n"
        "ds_read_b64 v[128:129], v3 offset:13352\n"
        "ds_read_b64 v[130:131], v3 offset:13560\n"
        "ds_read_b64 v[132:133], v3 offset:13768\n"
        "ds_read_b64 v[134:135], v3 offset:13976\n"
        "ds_read_b64 v[136:137], v3 offset:14184\n"
        "ds_read_b64 v[138:139], v3 offset:14392\n"
        "ds_read_b64 v[140:141], v3 offset:14600\n"
        "ds_read_b64 v[142:143], v3 offset:14808\n"
        "s_nop 0\n"
        "s_waitcnt lgkmcnt(8)\n"
        "v_perm_b32 v160, v146, v144, s47\n"
        "v_perm_b32 v164, v146, v144, s48\n"
        "v_perm_b32 v161, v150, v148, s47\n"
        "v_perm_b32 v165, v150, v148, s48\n"
        "v_perm_b32 v162, v154, v152, s47\n"
        "v_perm_b32 v166, v154, v152, s48\n"
        "v_perm_b32 v163, v158, v156, s47\n"
        "v_perm_b32 v167, v158, v156, s48\n"
        "v_perm_b32 v168, v147, v145, s47\n"
        "v_perm_b32 v172, v147, v145, s48\n"
        "v_perm_b32 v169, v151, v149, s47\n"
        "v_perm_b32 v173, v151, v149, s48\n"
        "v_perm_b32 v170, v155, v153, s47\n"
        "v_perm_b32 v174, v155, v153, s48\n"
        "v_perm_b32 v171, v159, v157, s47\n"
        "v_perm_b32 v175, v159, v157, s48\n"
        "ds_read_b64 v[112:113], v3 offset:20024\n"
        "ds_read_b64 v[114:115], v3 offset:20232\n"
        "ds_read_b64 v[116:117], v3 offset:20440\n"
        "ds_read_b64 v[118:119], v3 offset:20648\n"
        "ds_read_b64 v[120:121], v3 offset:20856\n"
        "ds_read_b64 v[122:123], v3 offset:21064\n"
        "ds_read_b64 v[124:125], v3 offset:21272\n"
        "ds_read_b64 v[126:127], v3 offset:21480\n"
        "s_waitcnt lgkmcnt(8)\n"
        "v_perm_b32 v144, v130, v128, s47\n"
        "v_perm_b32 v148, v130, v128, s48\n"
        "v_perm_b32 v145, v134, v132, s47\n"
        "v_perm_b32 v149, v134, v132, s48\n"
        "v_perm_b32 v146, v138, v136, s47\n"
        "v_perm_b32 v150, v138, v136, s48\n"
        "v_perm_b32 v147, v142, v140, s47\n"
        "v_perm_b32 v151, v142, v140, s48\n"
        "s_waitcnt vmcnt(8)\n"
        "v_perm_b32 v12, v20, v16, s47\n"
        "v_perm_b32 v13, v28, v24, s47\n"
        "v_perm_b32 v14, v20, v16, s48\n"
        "v_perm_b32 v15, v28, v24, s48\n"
        "ds_write_b64 v4, v[12:13] offset:0\n"
        "ds_write_b64 v4, v[14:15] offset:960\n"
        "v_perm_b32 v152, v131, v129, s47\n"
        "v_perm_b32 v156, v131, v129, s48\n"
        "v_perm_b32 v153, v135, v133, s47\n"
        "v_perm_b32 v157, v135, v133, s48\n"
        "v_perm_b32 v154, v139, v137, s47\n"
        "v_perm_b32 v158, v139, v137, s48\n"
        "v_perm_b32 v155, v143, v141, s47\n"
        "v_perm_b32 v159, v143, v141, s48\n"
        "v_perm_b32 v12, v21, v17, s47\n"
        "v_perm_b32 v13, v29, v25, s47\n"
        "v_perm_b32 v14, v21, v17, s48\n"
        "v_perm_b32 v15, v29, v25, s48\n"
        "ds_write_b64 v4, v[12:13] offset:1920\n"
        "ds_write_b64 v4, v[14:15] offset:2880\n"
        "ds_read_b64 v[96:97], v3 offset:16\n"
        "ds_read_b64 v[98:99], v3 offset:224\n"
        "ds_read_b64 v[100:101], v3 offset:432\n"
        "ds_read_b64 v[102:103], v3 offset:640\n"
        "ds_read_b64 v[104:105], v3 offset:848\n"
        "ds_read_b64 v[106:107], v3 offset:1056\n"
        "ds_read_b64 v[108:109], v3 offset:1264\n"
        "ds_read_b64 v[110:111], v3 offset:1472\n"
        "s_nop 0\n"
        "s_waitcnt lgkmcnt(8)\n"
        "v_perm_b32 v128, v114, v112, s47\n"
        "v_perm_b32 v132, v114, v112, s48\n"
        "v_perm_b32 v129, v118, v116, s47\n"
        "v_perm_b32 v133, v118, v116, s48\n"
        "v_perm_b32 v130, v122, v120, s47\n"
        "v_perm_b32 v134, v122, v120, s48\n"
        "v_perm_b32 v131, v126, v124, s47\n"
        "v_perm_b32 v135, v126, v124, s48\n"
        "v_perm_b32 v12, v22, v18, s47\n"
        "v_perm_b32 v13, v30, v26, s47\n"
        "v_perm_b32 v14, v22, v18, s48\n"
        "v_perm_b32 v15, v30, v26, s48\n"
        "ds_write_b64 v4, v[12:13] offset:3840\n"
        "ds_write_b64 v4, v[14:15] offset:4800\n"
        "v_perm_b32 v136, v115, v113, s47\n"
        "v_perm_b32 v140, v115, v113, s48\n"
        "v_perm_b32 v137, v119, v117, s47\n"
        "v_perm_b32 v141, v119, v117, s48\n"
        "v_perm_b32 v138, v123, v121, s47\n"
        "v_perm_b32 v142, v123, v121, s48\n"
        "v_perm_b32 v139, v127, v125, s47\n"
        "v_perm_b32 v143, v127, v125, s48\n"
        "v_perm_b32 v12, v23, v19, s47\n"
        "v_perm_b32 v13, v31, v27, s47\n"
        "v_perm_b32 v14, v23, v19, s48\n"
        "v_perm_b32 v15, v31, v27, s48\n"
        "ds_write_b64 v4, v[12:13] offset:5760\n"
        "ds_write_b64 v4, v[14:15] offset:6720\n"
        "ds_read_b64 v[80:81], v3 offset:6688\n"
        "ds_read_b64 v[82:83], v3 offset:6896\n"
        "ds_read_b64 v[84:85], v3 offset:7104\n"
        "ds_read_b64 v[86:87], v3 offset:7312\n"
        "ds_read_b64 v[88:89], v3 offset:7520\n"
        "ds_read_b64 v[90:91], v3 offset:7728\n"
        "ds_read_b64 v[92:93], v3 offset:7936\n"
        "ds_read_b64 v[94:95], v3 offset:8144\n"
        "s_waitcnt lgkmcnt(8)\n"
        "v_perm_b32 v112, v98, v96, s47\n"
        "v_perm_b32 v116, v98, v96, s48\n"
        "v_perm_b32 v113, v102, v100, s47\n"
        "v_perm_b32 v117, v102, v100, s48\n"
        "v_perm_b32 v114, v106, v104, s47\n"
        "v_perm_b32 v118, v106, v104, s48\n"
        "v_perm_b32 v115, v110, v108, s47\n"
        "v_perm_b32 v119, v110, v108, s48\n"
        "s_waitcnt vmcnt(4)\n"
        "v_perm_b32 v12, v36, v32, s47\n"
        "v_perm_b32 v13, v44, v40, s47\n"
        "v_perm_b32 v14, v36, v32, s48\n"
        "v_perm_b32 v15, v44, v40, s48\n"
        "ds_write_b64 v4, v[12:13] offset:192\n"
        "ds_write_b64 v4, v[14:15] offset:1152\n"
        "v_perm_b32 v120, v99, v97, s47\n"
        "v_perm_b32 v124, v99, v97, s48\n"
        "v_perm_b32 v121, v103, v101, s47\n"
        "v_perm_b32 v125, v103, v101, s48\n"
        "v_perm_b32 v122, v107, v105, s47\n"
        "v_perm_b32 v126, v107, v105, s48\n"
        "v_perm_b32 v123, v111, v109, s47\n"
        "v_perm_b32 v127, v111, v109, s48\n"
        "v_perm_b32 v12, v37, v33, s47\n"
        "v_perm_b32 v13, v45, v41, s47\n"
        "v_perm_b32 v14, v37, v33, s48\n"
        "v_perm_b32 v15, v45, v41, s48\n"
        "ds_write_b64 v4, v[12:13] offset:2112\n"
        "ds_write_b64 v4, v[14:15] offset:3072\n"
        "ds_read_b64 v[64:65], v3 offset:13360\n"
        "ds_read_b64 v[66:67], v3 offset:13568\n"
        "ds_read_b64 v[68:69], v3 offset:13776\n"
        "ds_read_b64 v[70:71], v3 offset:13984\n"
        "ds_read_b64 v[72:73], v3 offset:14192\n"
        "ds_read_b64 v[74:75], v3 offset:14400\n"
        "ds_read_b64 v[76:77], v3 offset:14608\n"
        "ds_read_b64 v[78:79], v3 offset:14816\n"
        "s_nop 0\n"
        "s_waitcnt lgkmcnt(8)\n"
        "v_perm_b32 v96, v82, v80, s47\n"
        "v_perm_b32 v100, v82, v80, s48\n"
        "v_perm_b32 v97, v86, v84, s47\n"
        "v_perm_b32 v101, v86, v84, s48\n"
        "v_perm_b32 v98, v90, v88, s47\n"
        "v_perm_b32 v102, v90, v88, s48\n"
        "v_perm_b32 v99, v94, v92, s47\n"
        "v_perm_b32 v103, v94, v92, s48\n"
        "v_perm_b32 v12, v38, v34, s47\n"
        "v_perm_b32 v13, v46, v42, s47\n"
        "v_perm_b32 v14, v38, v34, s48\n"
        "v_perm_b32 v15, v46, v42, s48\n"
        "ds_write_b64 v4, v[12:13] offset:4032\n"
        "ds_write_b64 v4, v[14:15] offset:4992\n"
        "v_perm_b32 v104, v83, v81, s47\n"
        "v_perm_b32 v108, v83, v81, s48\n"
        "v_perm_b32 v105, v87, v85, s47\n"
        "v_perm_b32 v109, v87, v85, s48\n"
        "v_perm_b32 v106, v91, v89, s47\n"
        "v_perm_b32 v110, v91, v89, s48\n"
        "v_perm_b32 v107, v95, v93, s47\n"
        "v_perm_b32 v111, v95, v93, s48\n"
        "v_perm_b32 v12, v39, v35, s47\n"
        "v_perm_b32 v13, v47, v43, s47\n"
        "v_perm_b32 v14, v39, v35, s48\n"
        "v_perm_b32 v15, v47, v43, s48\n"
        "ds_write_b64 v4, v[12:13] offset:5952\n"
        "ds_write_b64 v4, v[14:15] offset:6912\n"
        "s_nop 0\n"
        "s_waitcnt vmcnt(0)\n"
        "v_perm_b32 v16, v52, v48, s47\n"
        "v_perm_b32 v17, v60, v56, s47\n"
        "v_perm_b32 v18, v52, v48, s48\n"
        "v_perm_b32 v19, v60, v56, s48\n"
        "v_perm_b32 v20, v53, v49, s47\n"
        "v_perm_b32 v21, v61, v57, s47\n"
        "v_perm_b32 v22, v53, v49, s48\n"
        "v_perm_b32 v23, v61, v57, s48\n"
        "v_perm_b32 v24, v54, v50, s47\n"
        "v_perm_b32 v25, v62, v58, s47\n"
        "v_perm_b32 v26, v54, v50, s48\n"
        "v_perm_b32 v27, v62, v58, s48\n"
        "v_perm_b32 v28, v55, v51, s47\n"
        "v_perm_b32 v29, v63, v59, s47\n"
        "v_perm_b32 v30, v55, v51, s48\n"
        "v_perm_b32 v31, v63, v59, s48\n"
        "ds_read_b64 v[48:49], v3 offset:20032\n"
        "ds_read_b64 v[50:51], v3 offset:20240\n"
        "ds_read_b64 v[52:53], v3 offset:20448\n"
        "ds_read_b64 v[54:55], v3 offset:20656\n"
        "ds_read_b64 v[56:57], v3 offset:20864\n"
        "ds_read_b64 v[58:59], v3 offset:21072\n"
        "ds_read_b64 v[60:61], v3 offset:21280\n"
        "ds_read_b64 v[62:63], v3 offset:21488\n"
        "ds_write_b64 v4, v[16:17] offset:384\n"
        "ds_write_b64 v4, v[18:19] offset:1344\n"
        "ds_write_b64 v4, v[20:21] offset:2304\n"
        "ds_write_b64 v4, v[22:23] offset:3264\n"
        "s_nop 0\n"
        "s_waitcnt lgkmcnt(12)\n"
        "v_perm_b32 v80, v66, v64, s47\n"
        "v_perm_b32 v84, v66, v64, s48\n"
        "v_perm_b32 v81, v70, v68, s47\n"
        "v_perm_b32 v85, v70, v68, s48\n"
        "v_perm_b32 v82, v74, v72, s47\n"
        "v_perm_b32 v86, v74, v72, s48\n"
        "v_perm_b32 v83, v78, v76, s47\n"
        "v_perm_b32 v87, v78, v76, s48\n"
        "v_perm_b32 v88, v67, v65, s47\n"
        "v_perm_b32 v92, v67, v65, s48\n"
        "v_perm_b32 v89, v71, v69, s47\n"
        "v_perm_b32 v93, v71, v69, s48\n"
        "v_perm_b32 v90, v75, v73, s47\n"
        "v_perm_b32 v94, v75, v73, s48\n"
        "v_perm_b32 v91, v79, v77, s47\n"
        "v_perm_b32 v95, v79, v77, s48\n"
        "ds_write_b64 v4, v[24:25] offset:4224\n"
        "ds_write_b64 v4, v[26:27] offset:5184\n"
        "ds_write_b64 v4, v[28:29] offset:6144\n"
        "ds_write_b64 v4, v[30:31] offset:7104\n"
        "s_nop 0\n"
        "s_waitcnt lgkmcnt(4)\n"
        "v_perm_b32 v64, v50, v48, s47\n"
        "v_perm_b32 v68, v50, v48, s48\n"
        "v_perm_b32 v65, v54, v52, s47\n"
        "v_perm_b32 v69, v54, v52, s48\n"
        "v_perm_b32 v66, v58, v56, s47\n"
        "v_perm_b32 v70, v58, v56, s48\n"
        "v_perm_b32 v67, v62, v60, s47\n"
        "v_perm_b32 v71, v62, v60, s48\n"
        "v_perm_b32 v72, v51, v49, s47\n"
        "v_perm_b32 v76, v51, v49, s48\n"
        "v_perm_b32 v73, v55, v53, s47\n"
        "v_perm_b32 v77, v55, v53, s48\n"
        "v_perm_b32 v74, v59, v57, s47\n"
        "v_perm_b32 v78, v59, v57, s48\n"
        "v_perm_b32 v75, v63, v61, s47\n"
        "v_perm_b32 v79, v63, v61, s48\n"
        "s_lshr_b32 s23, %1, 3\n"
        "s_lshl_b32 s45, s23, 1\n"
        "s_and_b32 s23, s20, 1\n"
        "s_add_u32 s45, s45, s23\n"
        "s_mul_i32 s45, s45, 1152\n"
        "s_cmp_lt_u32 s20, 2\n"
        "s_cselect_b64 s[98:99], s[14:15], s[12:13]\n"
        "v_lshl_add_u32 v13, v2, 3, s45\n"
        "s_mov_b32 m0, 70656\n"
        "s_mov_b64 exec, 0x1ff\n"
        "global_load_lds_dword v13, s[98:99]\n"
        "s_mov_b64 exec, -1\n"
        ".L3_%=:\n"
        "s_cmp_ge_u32 s20, 4\n"
        "s_nop 0\n"
        "s_cbranch_scc0 .L4_%=\n"
        "ds_read_b64 v[224:225], v3 offset:0\n"
        "ds_read_b64 v[226:227], v3 offset:208\n"
        "ds_read_b64 v[228:229], v3 offset:416\n"
        "ds_read_b64 v[230:231], v3 offset:624\n"
        "ds_read_b64 v[232:233], v3 offset:832\n"
        "ds_read_b64 v[234:235], v3 offset:1040\n"
        "ds_read_b64 v[236:237], v3 offset:1248\n"
        "ds_read_b64 v[238:239], v3 offset:1456\n"
        "ds_read_b64 v[208:209], v3 offset:6672\n"
        "ds_read_b64 v[210:211], v3 offset:6880\n"
        "ds_read_b64 v[212:213], v3 offset:7088\n"
        "ds_read_b64 v[214:215], v3 offset:7296\n"
        "ds_read_b64 v[216:217], v3 offset:7504\n"
        "ds_read_b64 v[218:219], v3 offset:7712\n"
        "ds_read_b64 v[220:221], v3 offset:7920\n"
        "ds_read_b64 v[222:223], v3 offset:8128\n"
        "buffer_load_dwordx4 v[32:35], v6, s[24:27], 0 offen\n"
        "s_nop 0\n"
        "s_waitcnt lgkmcnt(8)\n"
        "v_perm_b32 v240, v226, v224, s47\n"
        "v_perm_b32 v244, v226, v224, s48\n"
        "v_perm_b32 v241, v230, v228, s47\n"
        "v_perm_b32 v245, v230, v228, s48\n"
        "v_perm_b32 v242, v234, v232, s47\n"
        "v_perm_b32 v246, v234, v232, s48\n"
        "v_perm_b32 v243, v238, v236, s47\n"
        "v_perm_b32 v247, v238, v236, s48\n"
        "v_perm_b32 v248, v227, v225, s47\n"
        "v_perm_b32 v252, v227, v225, s48\n"
        "v_perm_b32 v249, v231, v229, s47\n"
        "v_perm_b32 v253, v231, v229, s48\n"
        "v_perm_b32 v250, v235, v233, s47\n"
        "v_perm_b32 v254, v235, v233, s48\n"
        "v_perm_b32 v251, v239, v237, s47\n"
        "v_perm_b32 v255, v239, v237, s48\n"
        "ds_read_b64 v[192:193], v3 offset:13344\n"
        "ds_read_b64 v[194:195], v3 offset:13552\n"
        "ds_read_b64 v[196:197], v3 offset:13760\n"
        "ds_read_b64 v[198:199], v3 offset:13968\n"
        "ds_read_b64 v[200:201], v3 offset:14176\n"
        "ds_read_b64 v[202:203], v3 offset:14384\n"
        "ds_read_b64 v[204:205], v3 offset:14592\n"
        "ds_read_b64 v[206:207], v3 offset:14800\n"
        "buffer_load_dwordx4 v[36:39], v13, s[24:27], 0 offen\n"
        "s_nop 0\n"
        "s_waitcnt lgkmcnt(8)\n"
        "v_perm_b32 v224, v210, v208, s47\n"
        "v_perm_b32 v228, v210, v208, s48\n"
        "v_perm_b32 v225, v214, v212, s47\n"
        "v_perm_b32 v229, v214, v212, s48\n"
        "v_perm_b32 v226, v218, v216, s47\n"
        "v_perm_b32 v230, v218, v216, s48\n"
        "v_perm_b32 v227, v222, v220, s47\n"
        "v_perm_b32 v231, v222, v220, s48\n"
        "v_perm_b32 v232, v211, v209, s47\n"
        "v_perm_b32 v236, v211, v209, s48\n"
        "v_perm_b32 v233, v215, v213, s47\n"
        "v_perm_b32 v237, v215, v213, s48\n"
        "v_perm_b32 v234, v219, v217, s47\n"
        "v_perm_b32 v238, v219, v217, s48\n"
        "v_perm_b32 v235, v223, v221, s47\n"
        "v_perm_b32 v239, v223, v221, s48\n"
        "ds_read_b64 v[176:177], v3 offset:20016\n"
        "ds_read_b64 v[178:179], v3 offset:20224\n"
        "ds_read_b64 v[180:181], v3 offset:20432\n"
        "ds_read_b64 v[182:183], v3 offset:20640\n"
        "ds_read_b64 v[184:185], v3 offset:20848\n"
        "ds_read_b64 v[186:187], v3 offset:21056\n"
        "ds_read_b64 v[188:189], v3 offset:21264\n"
        "ds_read_b64 v[190:191], v3 offset:21472\n"
        "buffer_load_dwordx4 v[40:43], v14, s[24:27], 0 offen\n"
        "s_nop 0\n"
        "s_waitcnt lgkmcnt(8)\n"
        "v_perm_b32 v208, v194, v192, s47\n"
        "v_perm_b32 v212, v194, v192, s48\n"
        "v_perm_b32 v209, v198, v196, s47\n"
        "v_perm_b32 v213, v198, v196, s48\n"
        "v_perm_b32 v210, v202, v200, s47\n"
        "v_perm_b32 v214, v202, v200, s48\n"
        "v_perm_b32 v211, v206, v204, s47\n"
        "v_perm_b32 v215, v206, v204, s48\n"
        "v_perm_b32 v216, v195, v193, s47\n"
        "v_perm_b32 v220, v195, v193, s48\n"
        "v_perm_b32 v217, v199, v197, s47\n"
        "v_perm_b32 v221, v199, v197, s48\n"
        "v_perm_b32 v218, v203, v201, s47\n"
        "v_perm_b32 v222, v203, v201, s48\n"
        "v_perm_b32 v219, v207, v205, s47\n"
        "v_perm_b32 v223, v207, v205, s48\n"
        "ds_read_b64 v[160:161], v3 offset:8\n"
        "ds_read_b64 v[162:163], v3 offset:216\n"
        "ds_read_b64 v[164:165], v3 offset:424\n"
        "ds_read_b64 v[166:167], v3 offset:632\n"
        "ds_read_b64 v[168:169], v3 offset:840\n"
        "ds_read_b64 v[170:171], v3 offset:1048\n"
        "ds_read_b64 v[172:173], v3 offset:1256\n"
        "ds_read_b64 v[174:175], v3 offset:1464\n"
        "buffer_load_dwordx4 v[44:47], v15, s[24:27], 0 offen\n"
        "s_nop 0\n"
        "s_waitcnt lgkmcnt(8)\n"
        "v_perm_b32 v192, v178, v176, s47\n"
        "v_perm_b32 v196, v178, v176, s48\n"
        "v_perm_b32 v193, v182, v180, s47\n"
        "v_perm_b32 v197, v182, v180, s48\n"
        "v_perm_b32 v194, v186, v184, s47\n"
        "v_perm_b32 v198, v186, v184, s48\n"
        "v_perm_b32 v195, v190, v188, s47\n"
        "v_perm_b32 v199, v190, v188, s48\n"
        "v_perm_b32 v200, v179, v177, s47\n"
        "v_perm_b32 v204, v179, v177, s48\n"
        "v_perm_b32 v201, v183, v181, s47\n"
        "v_perm_b32 v205, v183, v181, s48\n"
        "v_perm_b32 v202, v187, v185, s47\n"
        "v_perm_b32 v206, v187, v185, s48\n"
        "v_perm_b32 v203, v191, v189, s47\n"
        "v_perm_b32 v207, v191, v189, s48\n"
        "ds_read_b64 v[144:145], v3 offset:6680\n"
        "ds_read_b64 v[146:147], v3 offset:6888\n"
        "ds_read_b64 v[148:149], v3 offset:7096\n"
        "ds_read_b64 v[150:151], v3 offset:7304\n"
        "ds_read_b64 v[152:153], v3 offset:7512\n"
        "ds_read_b64 v[154:155], v3 offset:7720\n"
        "ds_read_b64 v[156:157], v3 offset:7928\n"
        "ds_read_b64 v[158:159], v3 offset:8136\n"
        "s_lshl_b32 s23, s20, 4\n"
        "s_and_b32 s23, s23, 16\n"
        "v_add_u32 v6, s23, v5\n"
        "s_waitcnt lgkmcnt(8)\n"
        "v_perm_b32 v176, v162, v160, s47\n"
        "v_perm_b32 v180, v162, v160, s48\n"
        "v_perm_b32 v177, v166, v164, s47\n"
        "v_perm_b32 v181, v166, v164, s48\n"
        "v_perm_b32 v178, v170, v168, s47\n"
        "v_perm_b32 v182, v170, v168, s48\n"
        "v_perm_b32 v179, v174, v172, s47\n"
        "v_perm_b32 v183, v174, v172, s48\n"
        "v_perm_b32 v184, v163, v161, s47\n"
        "v_perm_b32 v188, v163, v161, s48\n"
        "v_perm_b32 v185, v167, v165, s47\n"
        "v_perm_b32 v189, v167, v165, s48\n"
        "v_perm_b32 v186, v171, v169, s47\n"
        "v_perm_b32 v190, v171, v169, s48\n"
        "v_perm_b32 v187, v175, v173, s47\n"
        "v_perm_b32 v191, v175, v173, s48\n"
        "ds_read_b64 v[128:129], v3 offset:13352\n"
        "ds_read_b64 v[130:131], v3 offset:13560\n"
        "ds_read_b64 v[132:133], v3 offset:13768\n"
        "ds_read_b64 v[134:135], v3 offset:13976\n"
        "ds_read_b64 v[136:137], v3 offset:14184\n"
        "ds_read_b64 v[138:139], v3 offset:14392\n"
        "ds_read_b64 v[140:141], v3 offset:14600\n"
        "ds_read_b64 v[142:143], v3 offset:14808\n"
        "s_nop 0\n"
        "s_waitcnt lgkmcnt(8)\n"
        "v_perm_b32 v160, v146, v144, s47\n"
        "v_perm_b32 v164, v146, v144, s48\n"
        "v_perm_b32 v161, v150, v148, s47\n"
        "v_perm_b32 v165, v150, v148, s48\n"
        "v_perm_b32 v162, v154, v152, s47\n"
        "v_perm_b32 v166, v154, v152, s48\n"
        "v_perm_b32 v163, v158, v156, s47\n"
        "v_perm_b32 v167, v158, v156, s48\n"
        "v_perm_b32 v168, v147, v145, s47\n"
        "v_perm_b32 v172, v147, v145, s48\n"
        "v_perm_b32 v169, v151, v149, s47\n"
        "v_perm_b32 v173, v151, v149, s48\n"
        "v_perm_b32 v170, v155, v153, s47\n"
        "v_perm_b32 v174, v155, v153, s48\n"
        "v_perm_b32 v171, v159, v157, s47\n"
        "v_perm_b32 v175, v159, v157, s48\n"
        "ds_read_b64 v[112:113], v3 offset:20024\n"
        "ds_read_b64 v[114:115], v3 offset:20232\n"
        "ds_read_b64 v[116:117], v3 offset:20440\n"
        "ds_read_b64 v[118:119], v3 offset:20648\n"
        "ds_read_b64 v[120:121], v3 offset:20856\n"
        "ds_read_b64 v[122:123], v3 offset:21064\n"
        "ds_read_b64 v[124:125], v3 offset:21272\n"
        "ds_read_b64 v[126:127], v3 offset:21480\n"
        "s_nop 0\n"
        "s_waitcnt lgkmcnt(8)\n"
        "v_perm_b32 v144, v130, v128, s47\n"
        "v_perm_b32 v148, v130, v128, s48\n"
        "v_perm_b32 v145, v134, v132, s47\n"
        "v_perm_b32 v149, v134, v132, s48\n"
        "v_perm_b32 v146, v138, v136, s47\n"
        "v_perm_b32 v150, v138, v136, s48\n"
        "v_perm_b32 v147, v142, v140, s47\n"
        "v_perm_b32 v151, v142, v140, s48\n"
        "v_perm_b32 v152, v131, v129, s47\n"
        "v_perm_b32 v156, v131, v129, s48\n"
        "v_perm_b32 v153, v135, v133, s47\n"
        "v_perm_b32 v157, v135, v133, s48\n"
        "v_perm_b32 v154, v139, v137, s47\n"
        "v_perm_b32 v158, v139, v137, s48\n"
        "v_perm_b32 v155, v143, v141, s47\n"
        "v_perm_b32 v159, v143, v141, s48\n"
        "s_nop 0\n"
        "s_waitcnt vmcnt(4)\n"
        "v_perm_b32 v12, v20, v16, s47\n"
        "v_perm_b32 v13, v28, v24, s47\n"
        "v_perm_b32 v14, v20, v16, s48\n"
        "v_perm_b32 v15, v28, v24, s48\n"
        "ds_write_b64 v4, v[12:13] offset:0\n"
        "ds_write_b64 v4, v[14:15] offset:960\n"
        "ds_read_b64 v[96:97], v3 offset:16\n"
        "ds_read_b64 v[98:99], v3 offset:224\n"
        "ds_read_b64 v[100:101], v3 offset:432\n"
        "ds_read_b64 v[102:103], v3 offset:640\n"
        "ds_read_b64 v[104:105], v3 offset:848\n"
        "ds_read_b64 v[106:107], v3 offset:1056\n"
        "ds_read_b64 v[108:109], v3 offset:1264\n"
        "ds_read_b64 v[110:111], v3 offset:1472\n"
        "s_nop 0\n"
        "s_waitcnt lgkmcnt(8)\n"
        "v_perm_b32 v128, v114, v112, s47\n"
        "v_perm_b32 v132, v114, v112, s48\n"
        "v_perm_b32 v129, v118, v116, s47\n"
        "v_perm_b32 v133, v118, v116, s48\n"
        "v_perm_b32 v130, v122, v120, s47\n"
        "v_perm_b32 v134, v122, v120, s48\n"
        "v_perm_b32 v131, v126, v124, s47\n"
        "v_perm_b32 v135, v126, v124, s48\n"
        "v_perm_b32 v136, v115, v113, s47\n"
        "v_perm_b32 v140, v115, v113, s48\n"
        "v_perm_b32 v137, v119, v117, s47\n"
        "v_perm_b32 v141, v119, v117, s48\n"
        "v_perm_b32 v138, v123, v121, s47\n"
        "v_perm_b32 v142, v123, v121, s48\n"
        "v_perm_b32 v139, v127, v125, s47\n"
        "v_perm_b32 v143, v127, v125, s48\n"
        "v_perm_b32 v12, v21, v17, s47\n"
        "v_perm_b32 v13, v29, v25, s47\n"
        "v_perm_b32 v14, v21, v17, s48\n"
        "v_perm_b32 v15, v29, v25, s48\n"
        "ds_write_b64 v4, v[12:13] offset:1920\n"
        "ds_write_b64 v4, v[14:15] offset:2880\n"
        "ds_read_b64 v[80:81], v3 offset:6688\n"
        "ds_read_b64 v[82:83], v3 offset:6896\n"
        "ds_read_b64 v[84:85], v3 offset:7104\n"
        "ds_read_b64 v[86:87], v3 offset:7312\n"
        "ds_read_b64 v[88:89], v3 offset:7520\n"
        "ds_read_b64 v[90:91], v3 offset:7728\n"
        "ds_read_b64 v[92:93], v3 offset:7936\n"
        "ds_read_b64 v[94:95], v3 offset:8144\n"
        "s_nop 0\n"
        "s_waitcnt lgkmcnt(8)\n"
        "v_perm_b32 v112, v98, v96, s47\n"
        "v_perm_b32 v116, v98, v96, s48\n"
        "v_perm_b32 v113, v102, v100, s47\n"
        "v_perm_b32 v117, v102, v100, s48\n"
        "v_perm_b32 v114, v106, v104, s47\n"
        "v_perm_b32 v118, v106, v104, s48\n"
        "v_perm_b32 v115, v110, v108, s47\n"
        "v_perm_b32 v119, v110, v108, s48\n"
        "v_perm_b32 v120, v99, v97, s47\n"
        "v_perm_b32 v124, v99, v97, s48\n"
        "v_perm_b32 v121, v103, v101, s47\n"
        "v_perm_b32 v125, v103, v101, s48\n"
        "v_perm_b32 v122, v107, v105, s47\n"
        "v_perm_b32 v126, v107, v105, s48\n"
        "v_perm_b32 v123, v111, v109, s47\n"
        "v_perm_b32 v127, v111, v109, s48\n"
        "v_perm_b32 v12, v22, v18, s47\n"
        "v_perm_b32 v13, v30, v26, s47\n"
        "v_perm_b32 v14, v22, v18, s48\n"
        "v_perm_b32 v15, v30, v26, s48\n"
        "ds_write_b64 v4, v[12:13] offset:3840\n"
        "ds_write_b64 v4, v[14:15] offset:4800\n"
        "ds_read_b64 v[64:65], v3 offset:13360\n"
        "ds_read_b64 v[66:67], v3 offset:13568\n"
        "ds_read_b64 v[68:69], v3 offset:13776\n"
        "ds_read_b64 v[70:71], v3 offset:13984\n"
        "ds_read_b64 v[72:73], v3 offset:14192\n"
        "ds_read_b64 v[74:75], v3 offset:14400\n"
        "ds_read_b64 v[76:77], v3 offset:14608\n"
        "ds_read_b64 v[78:79], v3 offset:14816\n"
        "s_nop 0\n"
        "s_waitcnt lgkmcnt(8)\n"
        "v_perm_b32 v96, v82, v80, s47\n"
        "v_perm_b32 v100, v82, v80, s48\n"
        "v_perm_b32 v97, v86, v84, s47\n"
        "v_perm_b32 v101, v86, v84, s48\n"
        "v_perm_b32 v98, v90, v88, s47\n"
        "v_perm_b32 v102, v90, v88, s48\n"
        "v_perm_b32 v99, v94, v92, s47\n"
        "v_perm_b32 v103, v94, v92, s48\n"
        "v_perm_b32 v104, v83, v81, s47\n"
        "v_perm_b32 v108, v83, v81, s48\n"
        "v_perm_b32 v105, v87, v85, s47\n"
        "v_perm_b32 v109, v87, v85, s48\n"
        "v_perm_b32 v106, v91, v89, s47\n"
        "v_perm_b32 v110, v91, v89, s48\n"
        "v_perm_b32 v107, v95, v93, s47\n"
        "v_perm_b32 v111, v95, v93, s48\n"
        "v_perm_b32 v12, v23, v19, s47\n"
        "v_perm_b32 v13, v31, v27, s47\n"
        "v_perm_b32 v14, v23, v19, s48\n"
        "v_perm_b32 v15, v31, v27, s48\n"
        "ds_write_b64 v4, v[12:13] offset:5760\n"
        "ds_write_b64 v4, v[14:15] offset:6720\n"
        "ds_read_b64 v[48:49], v3 offset:20032\n"
        "ds_read_b64 v[50:51], v3 offset:20240\n"
        "ds_read_b64 v[52:53], v3 offset:20448\n"
        "ds_read_b64 v[54:55], v3 offset:20656\n"
        "ds_read_b64 v[56:57], v3 offset:20864\n"
        "ds_read_b64 v[58:59], v3 offset:21072\n"
        "ds_read_b64 v[60:61], v3 offset:21280\n"
        "ds_read_b64 v[62:63], v3 offset:21488\n"
        "s_waitcnt lgkmcnt(8)\n"
        "v_perm_b32 v80, v66, v64, s47\n"
        "v_perm_b32 v84, v66, v64, s48\n"
        "v_perm_b32 v81, v70, v68, s47\n"
        "v_perm_b32 v85, v70, v68, s48\n"
        "v_perm_b32 v82, v74, v72, s47\n"
        "v_perm_b32 v86, v74, v72, s48\n"
        "v_perm_b32 v83, v78, v76, s47\n"
        "v_perm_b32 v87, v78, v76, s48\n"
        "s_waitcnt vmcnt(0)\n"
        "v_perm_b32 v12, v36, v32, s47\n"
        "v_perm_b32 v13, v44, v40, s47\n"
        "v_perm_b32 v14, v36, v32, s48\n"
        "v_perm_b32 v15, v44, v40, s48\n"
        "ds_write_b64 v4, v[12:13] offset:192\n"
        "ds_write_b64 v4, v[14:15] offset:1152\n"
        "v_perm_b32 v88, v67, v65, s47\n"
        "v_perm_b32 v92, v67, v65, s48\n"
        "v_perm_b32 v89, v71, v69, s47\n"
        "v_perm_b32 v93, v71, v69, s48\n"
        "v_perm_b32 v90, v75, v73, s47\n"
        "v_perm_b32 v94, v75, v73, s48\n"
        "v_perm_b32 v91, v79, v77, s47\n"
        "v_perm_b32 v95, v79, v77, s48\n"
        "v_perm_b32 v12, v37, v33, s47\n"
        "v_perm_b32 v13, v45, v41, s47\n"
        "v_perm_b32 v14, v37, v33, s48\n"
        "v_perm_b32 v15, v45, v41, s48\n"
        "ds_write_b64 v4, v[12:13] offset:2112\n"
        "ds_write_b64 v4, v[14:15] offset:3072\n"
        "s_nop 0\n"
        "s_waitcnt lgkmcnt(0)\n"
        "v_perm_b32 v64, v50, v48, s47\n"
        "v_perm_b32 v68, v50, v48, s48\n"
        "v_perm_b32 v65, v54, v52, s47\n"
        "v_perm_b32 v69, v54, v52, s48\n"
        "v_perm_b32 v66, v58, v56, s47\n"
        "v_perm_b32 v70, v58, v56, s48\n"
        "v_perm_b32 v67, v62, v60, s47\n"
        "v_perm_b32 v71, v62, v60, s48\n"
        "v_perm_b32 v12, v38, v34, s47\n"
        "v_perm_b32 v13, v46, v42, s47\n"
        "v_perm_b32 v14, v38, v34, s48\n"
        "v_perm_b32 v15, v46, v42, s48\n"
        "ds_write_b64 v4, v[12:13] offset:4032\n"
        "ds_write_b64 v4, v[14:15] offset:4992\n"
        "v_perm_b32 v72, v51, v49, s47\n"
        "v_perm_b32 v76, v51, v49, s48\n"
        "v_perm_b32 v73, v55, v53, s47\n"
        "v_perm_b32 v77, v55, v53, s48\n"
        "v_perm_b32 v74, v59, v57, s47\n"
        "v_perm_b32 v78, v59, v57, s48\n"
        "v_perm_b32 v75, v63, v61, s47\n"
        "v_perm_b32 v79, v63, v61, s48\n"
        "v_perm_b32 v12, v39, v35, s47\n"
        "v_perm_b32 v13, v47, v43, s47\n"
        "v_perm_b32 v14, v39, v35, s48\n"
        "v_perm_b32 v15, v47, v43, s48\n"
        "ds_write_b64 v4, v[12:13] offset:5952\n"
        "ds_write_b64 v4, v[14:15] offset:6912\n"
        ".L4_%=:\n"
        "s_cmp_lt_u32 s20, 4\n"
        "s_cbranch_scc0 .L5_%=\n"
        "s_and_b32 s44, s20, 3\n"
        "s_mov_b32 s28, s12\n"
        "s_and_b32 s29, s13, 0xffff\n"
        "s_mov_b32 s30, 73728\n"
        "s_mul_i32 s37, s44, 18432\n"
        "s_lshr_b32 s36, s44, 1\n"
        "s_mul_i32 s36, s36, 384\n"
        "s_add_u32 s38, s36, 128\n"
        "s_bitcmp1_b32 s44, 0\n"
        "s_cselect_b32 s23, 256, 0\n"
        "s_cselect_b32 s45, 36864, 0\n"
        "s_cselect_b32 s46, 6144, 0\n"
        "s_cselect_b32 s39, 0, 12288\n"
        "s_add_u32 s36, s36, s23\n"
        "s_add_u32 s38, s38, s45\n"
        "s_add_u32 s39, s39, s37\n"
        "s_add_u32 s37, s37, s46\n"
        ".L5_%=:\n"
        "s_waitcnt lgkmcnt(0)\n"
        "s_barrier\n"
        "ds_read_b128 v[16:19], v5 offset:0\n"
        "ds_read_b128 v[20:23], v5 offset:48\n"
        "ds_read_b128 v[24:27], v5 offset:96\n"
        "ds_read_b128 v[28:31], v5 offset:144\n"
        "ds_read_b128 v[32:35], v5 offset:960\n"
        "ds_read_b128 v[36:39], v5 offset:1008\n"
        "ds_read_b128 v[40:43], v5 offset:1056\n"
        "ds_read_b128 v[44:47], v5 offset:1104\n"
        "s_cmp_lt_u32 s20, 4\n"
        "s_cbranch_scc0 .L6_%=\n"
        "s_setprio 1\n"
        "s_waitcnt lgkmcnt(4)\n"
        "v_mfma_f32_16x16x32_bf16 v[12:15], v[240:243], v[16:19], 0\n"
        "ds_read_b128 v[16:19], v5 offset:1920\n"
        "s_mul_i32 s44, s20, 11776\n"
        "s_add_u32 s44, s44, 71424\n"
        "v_add_u32_e64 v4, s44, v2\n"
        "v_mfma_f32_16x16x32_bf16 v[12:15], v[224:227], v[20:23], v[12:15]\n"
        "ds_read_b128 v[20:23], v5 offset:1968\n"
        "s_add_u32 m0, s39, 0\n"
        "s_add_u32 s23, s38, 0\n"
        "buffer_load_dwordx4 v7, s[28:31], s23 offen offset:0 lds\n"
        "v_mfma_f32_16x16x32_bf16 v[12:15], v[208:211], v[24:27], v[12:15]\n"
        "ds_read_b128 v[24:27], v5 offset:2016\n"
        "v_mfma_f32_16x16x32_bf16 v[12:15], v[192:195], v[28:31], v[12:15]\n"
        "ds_read_b128 v[28:31], v5 offset:2064\n"
        "s_add_u32 s23, s38, 5120\n"
        "buffer_load_dwordx4 v7, s[28:31], s23 offen offset:1024 lds\n"
        "s_waitcnt lgkmcnt(4)\n"
        "v_mfma_f32_16x16x32_bf16 v[240:243], v[244:247], v[32:35], 0\n"
        "ds_read_b128 v[32:35], v5 offset:2880\n"
        "v_mfma_f32_16x16x32_bf16 v[240:243], v[228:231], v[36:39], v[240:243]\n"
        "ds_read_b128 v[36:39], v5 offset:2928\n"
        "s_add_u32 s23, s38, 10240\n"
        "buffer_load_dwordx4 v7, s[28:31], s23 offen offset:2048 lds\n"
        "v_mfma_f32_16x16x32_bf16 v[240:243], v[212:215], v[40:43], v[240:243]\n"
        "ds_read_b128 v[40:43], v5 offset:2976\n"
        "v_mfma_f32_16x16x32_bf16 v[240:243], v[196:199], v[44:47], v[240:243]\n"
        "ds_read_b128 v[44:47], v5 offset:3024\n"
        "s_add_u32 s23, s38, 15360\n"
        "buffer_load_dwordx4 v7, s[28:31], s23 offen offset:3072 lds\n"
        "s_waitcnt lgkmcnt(4)\n"
        "v_mfma_f32_16x16x32_bf16 v[224:227], v[248:251], v[16:19], 0\n"
        "ds_read_b128 v[16:19], v6 offset:3840\n"
        "v_mfma_f32_16x16x32_bf16 v[224:227], v[232:235], v[20:23], v[224:227]\n"
        "ds_read_b128 v[20:23], v6 offset:3888\n"
        "s_add_u32 m0, s39, 4096\n"
        "s_add_u32 s23, s38, 24576\n"
        "buffer_load_dwordx4 v7, s[28:31], s23 offen offset:0 lds\n"
        "v_mfma_f32_16x16x32_bf16 v[224:227], v[216:219], v[24:27], v[224:227]\n"
        "ds_read_b128 v[24:27], v6 offset:3936\n"
        "v_mfma_f32_16x16x32_bf16 v[224:227], v[200:203], v[28:31], v[224:227]\n"
        "ds_read_b128 v[28:31], v6 offset:3984\n"
        "s_add_u32 s23, s38, 29696\n"
        "buffer_load_dwordx4 v7, s[28:31], s23 offen offset:1024 lds\n"
        "s_waitcnt lgkmcnt(4)\n"
        "v_mfma_f32_16x16x32_bf16 v[208:211], v[252:255], v[32:35], 0\n"
        "ds_read_b128 v[32:35], v6 offset:4800\n"
        "v_add_f32 v192, v12, v240\n"
        "v_mfma_f32_16x16x32_bf16 v[208:211], v[236:239], v[36:39], v[208:211]\n"
        "ds_read_b128 v[36:39], v6 offset:4848\n"
        "s_add_u32 m0, s37, 0\n"
        "s_add_u32 s23, s36, 0\n"
        "buffer_load_dwordx4 v7, s[28:31], s23 offen offset:0 lds\n"
        "v_add_f32_e64 v193, v13, v241\n"
        "v_mfma_f32_16x16x32_bf16 v[208:211], v[220:223], v[40:43], v[208:211]\n"
        "ds_read_b128 v[40:43], v6 offset:4896\n"
        "v_add_f32_e64 v194, v14, v242\n"
        "v_mfma_f32_16x16x32_bf16 v[208:211], v[204:207], v[44:47], v[208:211]\n"
        "ds_read_b128 v[44:47], v6 offset:4944\n"
        "s_add_u32 s23, s36, 5120\n"
        "buffer_load_dwordx4 v7, s[28:31], s23 offen offset:1024 lds\n"
        "v_add_f32 v195, v15, v243\n"
        "s_waitcnt lgkmcnt(4)\n"
        "v_mfma_f32_16x16x32_bf16 v[244:247], v[176:179], v[16:19], 0\n"
        "ds_read_b128 v[16:19], v6 offset:5760\n"
        "v_add_f32_e64 v192, v192, v224\n"
        "v_mfma_f32_16x16x32_bf16 v[244:247], v[160:163], v[20:23], v[244:247]\n"
        "ds_read_b128 v[20:23], v6 offset:5808\n"
        "s_add_u32 s23, s36, 10240\n"
        "buffer_load_dwordx4 v7, s[28:31], s23 offen offset:2048 lds\n"
        "v_add_f32_e64 v193, v193, v225\n"
        "v_mfma_f32_16x16x32_bf16 v[244:247], v[144:147], v[24:27], v[244:247]\n"
        "ds_read_b128 v[24:27], v6 offset:5856\n"
        "v_add_f32_e64 v194, v194, v226\n"
        "v_mfma_f32_16x16x32_bf16 v[244:247], v[128:131], v[28:31], v[244:247]\n"
        "ds_read_b128 v[28:31], v6 offset:5904\n"
        "s_add_u32 s23, s36, 15360\n"
        "buffer_load_dwordx4 v7, s[28:31], s23 offen offset:3072 lds\n"
        "v_add_f32 v195, v195, v227\n"
        "s_waitcnt lgkmcnt(4)\n"
        "v_mfma_f32_16x16x32_bf16 v[228:231], v[180:183], v[32:35], 0\n"
        "ds_read_b128 v[32:35], v6 offset:6720\n"
        "v_add_f32_e64 v192, v192, v208\n"
        "v_mfma_f32_16x16x32_bf16 v[228:231], v[164:167], v[36:39], v[228:231]\n"
        "ds_read_b128 v[36:39], v6 offset:6768\n"
        "s_add_u32 m0, s37, 4096\n"
        "s_add_u32 s23, s36, 24576\n"
        "buffer_load_dwordx4 v7, s[28:31], s23 offen offset:0 lds\n"
        "v_add_f32_e64 v193, v193, v209\n"
        "v_mfma_f32_16x16x32_bf16 v[228:231], v[148:151], v[40:43], v[228:231]\n"
        "ds_read_b128 v[40:43], v6 offset:6816\n"
        "v_add_f32_e64 v194, v194, v210\n"
        "v_mfma_f32_16x16x32_bf16 v[228:231], v[132:135], v[44:47], v[228:231]\n"
        "ds_read_b128 v[44:47], v6 offset:6864\n"
        "s_add_u32 s23, s36, 29696\n"
        "buffer_load_dwordx4 v7, s[28:31], s23 offen offset:1024 lds\n"
        "v_add_f32 v195, v195, v211\n"
        "s_waitcnt lgkmcnt(4)\n"
        "v_mfma_f32_16x16x32_bf16 v[212:215], v[184:187], v[16:19], 0\n"
        "ds_read_b128 v[16:19], v5 offset:7696\n"
        "v_add_f32_e64 v192, v192, v244\n"
        "v_mfma_f32_16x16x32_bf16 v[212:215], v[168:171], v[20:23], v[212:215]\n"
        "ds_read_b128 v[20:23], v5 offset:7744\n"
        "s_add_u32 s23, s36, 34816\n"
        "buffer_load_dwordx4 v7, s[28:31], s23 offen offset:2048 lds\n"
        "v_add_f32_e64 v193, v193, v245\n"
        "v_mfma_f32_16x16x32_bf16 v[212:215], v[152:155], v[24:27], v[212:215]\n"
        "ds_read_b128 v[24:27], v5 offset:7792\n"
        "v_add_f32_e64 v194, v194, v246\n"
        "v_mfma_f32_16x16x32_bf16 v[212:215], v[136:139], v[28:31], v[212:215]\n"
        "ds_read_b128 v[28:31], v5 offset:7840\n"
        "s_add_u32 s23, s36, 39936\n"
        "buffer_load_dwordx4 v7, s[28:31], s23 offen offset:3072 lds\n"
        "v_add_f32 v195, v195, v247\n"
        "s_waitcnt lgkmcnt(4)\n"
        "v_mfma_f32_16x16x32_bf16 v[248:251], v[188:191], v[32:35], 0\n"
        "ds_read_b128 v[32:35], v5 offset:8656\n"
        "v_add_f32_e64 v192, v192, v228\n"
        "v_mfma_f32_16x16x32_bf16 v[248:251], v[172:175], v[36:39], v[248:251]\n"
        "ds_read_b128 v[36:39], v5 offset:8704\n"
        "s_add_u32 m0, s37, 8192\n"
        "s_add_u32 s23, s36, 49152\n"
        "buffer_load_dwordx4 v7, s[28:31], s23 offen offset:0 lds\n"
        "v_add_f32_e64 v193, v193, v229\n"
        "v_mfma_f32_16x16x32_bf16 v[248:251], v[156:159], v[40:43], v[248:251]\n"
        "ds_read_b128 v[40:43], v5 offset:8752\n"
        "v_add_f32_e64 v194, v194, v230\n"
        "v_mfma_f32_16x16x32_bf16 v[248:251], v[140:143], v[44:47], v[248:251]\n"
        "ds_read_b128 v[44:47], v5 offset:8800\n"
        "v_add_f32 v195, v195, v231\n"
        "s_waitcnt lgkmcnt(4)\n"
        "v_mfma_f32_16x16x32_bf16 v[232:235], v[112:115], v[16:19], 0\n"
        "ds_read_b128 v[16:19], v5 offset:9616\n"
        "v_add_f32_e64 v192, v192, v212\n"
        "v_mfma_f32_16x16x32_bf16 v[232:235], v[96:99], v[20:23], v[232:235]\n"
        "ds_read_b128 v[20:23], v5 offset:9664\n"
        "v_add_f32_e64 v193, v193, v213\n"
        "v_mfma_f32_16x16x32_bf16 v[232:235], v[80:83], v[24:27], v[232:235]\n"
        "ds_read_b128 v[24:27], v5 offset:9712\n"
        "v_add_f32_e64 v194, v194, v214\n"
        "v_mfma_f32_16x16x32_bf16 v[232:235], v[64:67], v[28:31], v[232:235]\n"
        "ds_read_b128 v[28:31], v5 offset:9760\n"
        "v_add_f32 v195, v195, v215\n"
        "s_waitcnt lgkmcnt(4)\n"
        "v_mfma_f32_16x16x32_bf16 v[216:219], v[116:119], v[32:35], 0\n"
        "ds_read_b128 v[32:35], v5 offset:10576\n"
        "v_add_f32_e64 v192, v192, v248\n"
        "v_mfma_f32_16x16x32_bf16 v[216:219], v[100:103], v[36:39], v[216:219]\n"
        "ds_read_b128 v[36:39], v5 offset:10624\n"
        "v_add_f32_e64 v193, v193, v249\n"
        "v_mfma_f32_16x16x32_bf16 v[216:219], v[84:87], v[40:43], v[216:219]\n"
        "ds_read_b128 v[40:43], v5 offset:10672\n"
        "v_add_f32_e64 v194, v194, v250\n"
        "v_mfma_f32_16x16x32_bf16 v[216:219], v[68:71], v[44:47], v[216:219]\n"
        "ds_read_b128 v[44:47], v5 offset:10720\n"
        "v_add_f32 v195, v195, v251\n"
        "s_waitcnt lgkmcnt(4)\n"
        "v_mfma_f32_16x16x32_bf16 v[252:255], v[120:123], v[16:19], 0\n"
        "v_add_f32_e64 v192, v192, v232\n"
        "v_mfma_f32_16x16x32_bf16 v[252:255], v[104:107], v[20:23], v[252:255]\n"
        "v_add_f32_e64 v193, v193, v233\n"
        "v_mfma_f32_16x16x32_bf16 v[252:255], v[88:91], v[24:27], v[252:255]\n"
        "v_add_f32_e64 v194, v194, v234\n"
        "v_mfma_f32_16x16x32_bf16 v[252:255], v[72:75], v[28:31], v[252:255]\n"
        "v_add_f32 v195, v195, v235\n"
        "s_waitcnt lgkmcnt(0)\n"
        "v_mfma_f32_16x16x32_bf16 v[236:239], v[124:127], v[32:35], 0\n"
        "v_add_f32_e64 v192, v192, v216\n"
        "v_mfma_f32_16x16x32_bf16 v[236:239], v[108:111], v[36:39], v[236:239]\n"
        "v_add_f32_e64 v193, v193, v217\n"
        "v_mfma_f32_16x16x32_bf16 v[236:239], v[92:95], v[40:43], v[236:239]\n"
        "v_add_f32_e64 v194, v194, v218\n"
        "v_mfma_f32_16x16x32_bf16 v[236:239], v[76:79], v[44:47], v[236:239]\n"
        "v_add_f32 v195, v195, v219\n"
        "s_setprio 0\n"
        "s_cmp_lt_u32 s20, 3\n"
        "s_cbranch_scc0 .L7_%=\n"
        "s_add_u32 s23, s36, 54272\n"
        "buffer_load_dwordx4 v7, s[28:31], s23 offen offset:1024 lds\n"
        "s_add_u32 s23, s36, 59392\n"
        "buffer_load_dwordx4 v7, s[28:31], s23 offen offset:2048 lds\n"
        "s_add_u32 s23, s36, 64512\n"
        "buffer_load_dwordx4 v7, s[28:31], s23 offen offset:3072 lds\n"
        ".L7_%=:\n"
        "v_mul_f32 v196, v12, v12\n"
        "v_mul_f32 v197, v13, v13\n"
        "v_mul_f32 v198, v14, v14\n"
        "v_mul_f32 v199, v15, v15\n"
        "v_fmac_f32 v196, v240, v240\n"
        "v_fmac_f32 v197, v241, v241\n"
        "v_fmac_f32 v198, v242, v242\n"
        "v_fmac_f32 v199, v243, v243\n"
        "v_fmac_f32 v196, v224, v224\n"
        "v_fmac_f32 v197, v225, v225\n"
        "v_fmac_f32 v198, v226, v226\n"
        "v_fmac_f32 v199, v227, v227\n"
        "v_fmac_f32 v196, v208, v208\n"
        "v_fmac_f32 v197, v209, v209\n"
        "v_fmac_f32 v198, v210, v210\n"
        "v_fmac_f32 v199, v211, v211\n"
        "v_fmac_f32 v196, v244, v244\n"
        "v_fmac_f32 v197, v245, v245\n"
        "v_fmac_f32 v198, v246, v246\n"
        "v_fmac_f32 v199, v247, v247\n"
        "v_fmac_f32 v196, v228, v228\n"
        "v_fmac_f32 v197, v229, v229\n"
        "v_fmac_f32 v198, v230, v230\n"
        "v_fmac_f32 v199, v231, v231\n"
        "v_fmac_f32 v196, v212, v212\n"
        "v_fmac_f32 v197, v213, v213\n"
        "v_fmac_f32 v198, v214, v214\n"
        "v_fmac_f32 v199, v215, v215\n"
        "v_fmac_f32 v196, v248, v248\n"
        "v_fmac_f32 v197, v249, v249\n"
        "v_fmac_f32 v198, v250, v250\n"
        "v_fmac_f32 v199, v251, v251\n"
        "v_fmac_f32 v196, v232, v232\n"
        "v_fmac_f32 v197, v233, v233\n"
        "v_fmac_f32 v198, v234, v234\n"
        "v_fmac_f32 v199, v235, v235\n"
        "v_fmac_f32 v196, v216, v216\n"
        "v_fmac_f32 v197, v217, v217\n"
        "v_fmac_f32 v198, v218, v218\n"
        "v_fmac_f32 v199, v219, v219\n"
        "v_fmac_f32 v196, v252, v252\n"
        "v_fmac_f32 v197, v253, v253\n"
        "v_fmac_f32 v198, v254, v254\n"
        "v_fmac_f32 v199, v255, v255\n"
        "v_fmac_f32 v196, v236, v236\n"
        "v_fmac_f32 v197, v237, v237\n"
        "v_fmac_f32 v198, v238, v238\n"
        "v_fmac_f32 v199, v239, v239\n"
        "v_add_f32 v192, v192, v252\n"
        "v_add_f32 v193, v193, v253\n"
        "v_add_f32 v194, v194, v254\n"
        "v_add_f32 v195, v195, v255\n"
        "v_add_f32 v192, v192, v236\n"
        "v_add_f32 v193, v193, v237\n"
        "v_add_f32 v194, v194, v238\n"
        "v_add_f32 v195, v195, v239\n"
        "v_mov_b32_e64 v5, v8\n"
        "v_add_u32 v3, 256, v8\n"
        ".L6_%=:\n"
        "s_cmp_ge_u32 s20, 4\n"
        "s_cbranch_scc0 .L8_%=\n"
        "s_setprio 1\n"
        "s_waitcnt lgkmcnt(4)\n"
        "v_mfma_f32_16x16x32_bf16 v[12:15], v[240:243], v[16:19], 0\n"
        "ds_read_b128 v[16:19], v5 offset:1920\n"
        "s_mul_i32 s44, s20, 11776\n"
        "s_add_u32 s44, s44, 71424\n"
        "v_add_u32_e64 v4, s44, v2\n"
        "v_mfma_f32_16x16x32_bf16 v[12:15], v[224:227], v[20:23], v[12:15]\n"
        "ds_read_b128 v[20:23], v5 offset:1968\n"
        "v_mfma_f32_16x16x32_bf16 v[12:15], v[208:211], v[24:27], v[12:15]\n"
        "ds_read_b128 v[24:27], v5 offset:2016\n"
        "v_mfma_f32_16x16x32_bf16 v[12:15], v[192:195], v[28:31], v[12:15]\n"
        "ds_read_b128 v[28:31], v5 offset:2064\n"
        "s_waitcnt lgkmcnt(4)\n"
        "v_mfma_f32_16x16x32_bf16 v[240:243], v[244:247], v[32:35], 0\n"
        "ds_read_b128 v[32:35], v5 offset:2880\n"
        "v_mfma_f32_16x16x32_bf16 v[240:243], v[228:231], v[36:39], v[240:243]\n"
        "ds_read_b128 v[36:39], v5 offset:2928\n"
        "v_mfma_f32_16x16x32_bf16 v[240:243], v[212:215], v[40:43], v[240:243]\n"
        "ds_read_b128 v[40:43], v5 offset:2976\n"
        "v_mfma_f32_16x16x32_bf16 v[240:243], v[196:199], v[44:47], v[240:243]\n"
        "ds_read_b128 v[44:47], v5 offset:3024\n"
        "s_waitcnt lgkmcnt(4)\n"
        "v_mfma_f32_16x16x32_bf16 v[224:227], v[248:251], v[16:19], 0\n"
        "ds_read_b128 v[16:19], v6 offset:3840\n"
        "v_mfma_f32_16x16x32_bf16 v[224:227], v[232:235], v[20:23], v[224:227]\n"
        "ds_read_b128 v[20:23], v6 offset:3888\n"
        "v_mfma_f32_16x16x32_bf16 v[224:227], v[216:219], v[24:27], v[224:227]\n"
        "ds_read_b128 v[24:27], v6 offset:3936\n"
        "v_mfma_f32_16x16x32_bf16 v[224:227], v[200:203], v[28:31], v[224:227]\n"
        "ds_read_b128 v[28:31], v6 offset:3984\n"
        "s_waitcnt lgkmcnt(4)\n"
        "v_mfma_f32_16x16x32_bf16 v[208:211], v[252:255], v[32:35], 0\n"
        "ds_read_b128 v[32:35], v6 offset:4800\n"
        "v_add_f32 v192, v12, v240\n"
        "v_mfma_f32_16x16x32_bf16 v[208:211], v[236:239], v[36:39], v[208:211]\n"
        "ds_read_b128 v[36:39], v6 offset:4848\n"
        "v_add_f32_e64 v193, v13, v241\n"
        "v_mfma_f32_16x16x32_bf16 v[208:211], v[220:223], v[40:43], v[208:211]\n"
        "ds_read_b128 v[40:43], v6 offset:4896\n"
        "v_add_f32_e64 v194, v14, v242\n"
        "v_mfma_f32_16x16x32_bf16 v[208:211], v[204:207], v[44:47], v[208:211]\n"
        "ds_read_b128 v[44:47], v6 offset:4944\n"
        "v_add_f32 v195, v15, v243\n"
        "s_waitcnt lgkmcnt(4)\n"
        "v_mfma_f32_16x16x32_bf16 v[244:247], v[176:179], v[16:19], 0\n"
        "ds_read_b128 v[16:19], v6 offset:5760\n"
        "v_add_f32_e64 v192, v192, v224\n"
        "v_mfma_f32_16x16x32_bf16 v[244:247], v[160:163], v[20:23], v[244:247]\n"
        "ds_read_b128 v[20:23], v6 offset:5808\n"
        "v_add_f32_e64 v193, v193, v225\n"
        "v_mfma_f32_16x16x32_bf16 v[244:247], v[144:147], v[24:27], v[244:247]\n"
        "ds_read_b128 v[24:27], v6 offset:5856\n"
        "v_add_f32_e64 v194, v194, v226\n"
        "v_mfma_f32_16x16x32_bf16 v[244:247], v[128:131], v[28:31], v[244:247]\n"
        "ds_read_b128 v[28:31], v6 offset:5904\n"
        "v_add_f32 v195, v195, v227\n"
        "s_waitcnt lgkmcnt(4)\n"
        "v_mfma_f32_16x16x32_bf16 v[228:231], v[180:183], v[32:35], 0\n"
        "ds_read_b128 v[32:35], v6 offset:6720\n"
        "v_add_f32_e64 v192, v192, v208\n"
        "v_mfma_f32_16x16x32_bf16 v[228:231], v[164:167], v[36:39], v[228:231]\n"
        "ds_read_b128 v[36:39], v6 offset:6768\n"
        "v_add_f32_e64 v193, v193, v209\n"
        "v_mfma_f32_16x16x32_bf16 v[228:231], v[148:151], v[40:43], v[228:231]\n"
        "ds_read_b128 v[40:43], v6 offset:6816\n"
        "v_add_f32_e64 v194, v194, v210\n"
        "v_mfma_f32_16x16x32_bf16 v[228:231], v[132:135], v[44:47], v[228:231]\n"
        "ds_read_b128 v[44:47], v6 offset:6864\n"
        "v_add_f32 v195, v195, v211\n"
        "s_waitcnt lgkmcnt(4)\n"
        "v_mfma_f32_16x16x32_bf16 v[212:215], v[184:187], v[16:19], 0\n"
        "ds_read_b128 v[16:19], v5 offset:7696\n"
        "v_add_f32_e64 v192, v192, v244\n"
        "v_mfma_f32_16x16x32_bf16 v[212:215], v[168:171], v[20:23], v[212:215]\n"
        "ds_read_b128 v[20:23], v5 offset:7744\n"
        "v_add_f32_e64 v193, v193, v245\n"
        "v_mfma_f32_16x16x32_bf16 v[212:215], v[152:155], v[24:27], v[212:215]\n"
        "ds_read_b128 v[24:27], v5 offset:7792\n"
        "v_add_f32_e64 v194, v194, v246\n"
        "v_mfma_f32_16x16x32_bf16 v[212:215], v[136:139], v[28:31], v[212:215]\n"
        "ds_read_b128 v[28:31], v5 offset:7840\n"
        "v_add_f32 v195, v195, v247\n"
        "s_waitcnt lgkmcnt(4)\n"
        "v_mfma_f32_16x16x32_bf16 v[248:251], v[188:191], v[32:35], 0\n"
        "ds_read_b128 v[32:35], v5 offset:8656\n"
        "v_add_f32_e64 v192, v192, v228\n"
        "v_mfma_f32_16x16x32_bf16 v[248:251], v[172:175], v[36:39], v[248:251]\n"
        "ds_read_b128 v[36:39], v5 offset:8704\n"
        "v_add_f32_e64 v193, v193, v229\n"
        "v_mfma_f32_16x16x32_bf16 v[248:251], v[156:159], v[40:43], v[248:251]\n"
        "ds_read_b128 v[40:43], v5 offset:8752\n"
        "v_add_f32_e64 v194, v194, v230\n"
        "v_mfma_f32_16x16x32_bf16 v[248:251], v[140:143], v[44:47], v[248:251]\n"
        "ds_read_b128 v[44:47], v5 offset:8800\n"
        "v_add_f32 v195, v195, v231\n"
        "s_waitcnt lgkmcnt(4)\n"
        "v_mfma_f32_16x16x32_bf16 v[232:235], v[112:115], v[16:19], 0\n"
        "ds_read_b128 v[16:19], v5 offset:9616\n"
        "v_add_f32_e64 v192, v192, v212\n"
        "v_mfma_f32_16x16x32_bf16 v[232:235], v[96:99], v[20:23], v[232:235]\n"
        "ds_read_b128 v[20:23], v5 offset:9664\n"
        "v_add_f32_e64 v193, v193, v213\n"
        "v_mfma_f32_16x16x32_bf16 v[232:235], v[80:83], v[24:27], v[232:235]\n"
        "ds_read_b128 v[24:27], v5 offset:9712\n"
        "v_add_f32_e64 v194, v194, v214\n"
        "v_mfma_f32_16x16x32_bf16 v[232:235], v[64:67], v[28:31], v[232:235]\n"
        "ds_read_b128 v[28:31], v5 offset:9760\n"
        "v_add_f32 v195, v195, v215\n"
        "s_waitcnt lgkmcnt(4)\n"
        "v_mfma_f32_16x16x32_bf16 v[216:219], v[116:119], v[32:35], 0\n"
        "ds_read_b128 v[32:35], v5 offset:10576\n"
        "v_add_f32_e64 v192, v192, v248\n"
        "v_mfma_f32_16x16x32_bf16 v[216:219], v[100:103], v[36:39], v[216:219]\n"
        "ds_read_b128 v[36:39], v5 offset:10624\n"
        "v_add_f32_e64 v193, v193, v249\n"
        "v_mfma_f32_16x16x32_bf16 v[216:219], v[84:87], v[40:43], v[216:219]\n"
        "ds_read_b128 v[40:43], v5 offset:10672\n"
        "v_add_f32_e64 v194, v194, v250\n"
        "v_mfma_f32_16x16x32_bf16 v[216:219], v[68:71], v[44:47], v[216:219]\n"
        "ds_read_b128 v[44:47], v5 offset:10720\n"
        "v_add_f32 v195, v195, v251\n"
        "s_waitcnt lgkmcnt(4)\n"
        "v_mfma_f32_16x16x32_bf16 v[252:255], v[120:123], v[16:19], 0\n"
        "v_add_f32_e64 v192, v192, v232\n"
        "v_mfma_f32_16x16x32_bf16 v[252:255], v[104:107], v[20:23], v[252:255]\n"
        "v_add_f32_e64 v193, v193, v233\n"
        "v_mfma_f32_16x16x32_bf16 v[252:255], v[88:91], v[24:27], v[252:255]\n"
        "v_add_f32_e64 v194, v194, v234\n"
        "v_mfma_f32_16x16x32_bf16 v[252:255], v[72:75], v[28:31], v[252:255]\n"
        "v_add_f32 v195, v195, v235\n"
        "s_waitcnt lgkmcnt(0)\n"
        "v_mfma_f32_16x16x32_bf16 v[236:239], v[124:127], v[32:35], 0\n"
        "v_add_f32_e64 v192, v192, v216\n"
        "v_mfma_f32_16x16x32_bf16 v[236:239], v[108:111], v[36:39], v[236:239]\n"
        "v_add_f32_e64 v193, v193, v217\n"
        "v_mfma_f32_16x16x32_bf16 v[236:239], v[92:95], v[40:43], v[236:239]\n"
        "v_add_f32_e64 v194, v194, v218\n"
        "v_mfma_f32_16x16x32_bf16 v[236:239], v[76:79], v[44:47], v[236:239]\n"
        "v_add_f32_e64 v195, v195, v219\n"
        "v_pk_mul_f32 v[196:197], v[12:13], v[12:13]\n"
        "v_pk_mul_f32 v[198:199], v[14:15], v[14:15]\n"
        "v_pk_mul_f32 v[200:201], v[240:241], v[240:241]\n"
        "v_pk_mul_f32 v[202:203], v[242:243], v[242:243]\n"
        "v_pk_fma_f32 v[196:197], v[224:225], v[224:225], v[196:197]\n"
        "v_pk_fma_f32 v[198:199], v[226:227], v[226:227], v[198:199]\n"
        "v_pk_fma_f32 v[200:201], v[208:209], v[208:209], v[200:201]\n"
        "v_pk_fma_f32 v[202:203], v[210:211], v[210:211], v[202:203]\n"
        "v_pk_fma_f32 v[196:197], v[244:245], v[244:245], v[196:197]\n"
        "v_pk_fma_f32 v[198:199], v[246:247], v[246:247], v[198:199]\n"
        "v_pk_fma_f32 v[200:201], v[228:229], v[228:229], v[200:201]\n"
        "v_pk_fma_f32 v[202:203], v[230:231], v[230:231], v[202:203]\n"
        "v_pk_fma_f32 v[196:197], v[212:213], v[212:213], v[196:197]\n"
        "v_pk_fma_f32 v[198:199], v[214:215], v[214:215], v[198:199]\n"
        "v_pk_fma_f32 v[200:201], v[248:249], v[248:249], v[200:201]\n"
        "v_pk_fma_f32 v[202:203], v[250:251], v[250:251], v[202:203]\n"
        "v_pk_fma_f32 v[196:197], v[232:233], v[232:233], v[196:197]\n"
        "v_pk_fma_f32 v[198:199], v[234:235], v[234:235], v[198:199]\n"
        "v_pk_add_f32 v[192:193], v[192:193], v[252:253]\n"
        "v_pk_add_f32 v[194:195], v[194:195], v[254:255]\n"
        "v_pk_fma_f32 v[200:201], v[216:217], v[216:217], v[200:201]\n"
        "v_pk_fma_f32 v[202:203], v[218:219], v[218:219], v[202:203]\n"
        "v_pk_fma_f32 v[196:197], v[252:253], v[252:253], v[196:197]\n"
        "v_pk_fma_f32 v[198:199], v[254:255], v[254:255], v[198:199]\n"
        "v_pk_add_f32 v[192:193], v[192:193], v[236:237]\n"
        "v_pk_add_f32 v[194:195], v[194:195], v[238:239]\n"
        "v_pk_fma_f32 v[200:201], v[236:237], v[236:237], v[200:201]\n"
        "v_pk_fma_f32 v[202:203], v[238:239], v[238:239], v[202:203]\n"
        "v_pk_add_f32 v[196:197], v[196:197], v[200:201]\n"
        "v_pk_add_f32 v[198:199], v[198:199], v[202:203]\n"
        ".L8_%=:\n"
        "s_setprio 0\n"
        "ds_write_b128 v4, v[192:195]\n"
        "ds_write_b128 v4, v[196:199] offset:1024\n"
        "v_mov_b64 v[48:49], 0\n"
        "v_mov_b64 v[50:51], 0\n"
        "v_add_u32 v4, 97024, v2\n"
        "v_add_u32 v24, 47104, v11\n"
        "s_waitcnt vmcnt(0)\n"
        "s_waitcnt lgkmcnt(0)\n"
        "s_barrier\n"
        "ds_read2st64_b32 v[16:17], v11 offset0:0 offset1:46\n"
        "ds_read2st64_b32 v[18:19], v11 offset0:92 offset1:138\n"
        "ds_read2st64_b32 v[20:21], v24 offset0:0 offset1:46\n"
        "ds_read2st64_b32 v[22:23], v24 offset0:92 offset1:138\n"
        "s_cmp_ge_u32 s20, 4\n"
        "s_cbranch_scc0 .L9_%=\n"
        "v_mfma_f32_32x32x16_bf16 v[64:79], v[48:51], v[48:51], 0\n"
        "s_bitcmp1_b32 s49, 11\n"
        "s_cselect_b32 s23, 5888, 0\n"
        "s_sub_u32 s45, s49, s23\n"
        "v_add_u32 v5, s45, v8\n"
        "s_lshl_b32 s23, s23, 1\n"
        "s_add_u32 s23, s23, 256\n"
        "v_add_u32_e64 v3, s23, v5\n"
        "v_mfma_f32_32x32x16_bf16 v[80:95], v[48:51], v[48:51], 0\n"
        ".L9_%=:\n"
        "s_waitcnt lgkmcnt(0)\n"
        "v_add_f32 v16, v16, v17\n"
        "v_add_f32 v18, v18, v19\n"
        "v_add_f32 v20, v20, v21\n"
        "v_add_f32 v22, v22, v23\n"
        "v_add_f32 v16, v16, v18\n"
        "v_add_f32_e64 v20, v20, v22\n"
        "v_add_u32 v18, 25600, v11\n"
        "v_add_f32_e64 v16, v16, v20\n"
        "v_mul_f32 v17, 0x3c2aaaab, v16\n"
        "v_mul_f32 v16, 0x3c2aaaab, v16\n"
        "v_mov_b32 v52, 163776\n"
        "v_mov_b32 v53, 1\n"
        "v_permlane32_swap_b32 v16, v17\n"
        "v_fma_f32 v17, -v16, v16, v17\n"
        "v_max_f32_e64 v17, 0, v17\n"
        "v_add_f32 v17, 0x3727c5ac, v17\n"
        "v_rsq_f32 v17, v17\n"
        "v_mul_f32 v16, s56, v16\n"
        "s_mov_b32 exec_hi, 0\n"
        "v_mov_b32 v16, 0xbeeff558\n"
        "s_mov_b32 exec_hi, -1\n"
        "v_mul_f32_e64 v17, v17, -v16\n"
        "ds_write_b32 v18, v17\n"
        "s_cmp_lt_u32 s20, 4\n"
        "s_cbranch_scc0 .L10_%=\n"
        "ds_read_b64_tr_b16 v[184:185], v5 offset:0\n"
        "ds_read_b64_tr_b16 v[186:187], v5 offset:512\n"
        "ds_read_b64_tr_b16 v[188:189], v5 offset:2048\n"
        "ds_read_b64_tr_b16 v[190:191], v5 offset:2560\n"
        "ds_read_b64_tr_b16 v[192:193], v5 offset:4096\n"
        "ds_read_b64_tr_b16 v[194:195], v5 offset:4608\n"
        "ds_read_b64_tr_b16 v[196:197], v5 offset:6144\n"
        "ds_read_b64_tr_b16 v[198:199], v5 offset:6656\n"
        "ds_read_b64_tr_b16 v[200:201], v5 offset:8192\n"
        "ds_read_b64_tr_b16 v[202:203], v5 offset:8704\n"
        "ds_read_b64_tr_b16 v[204:205], v5 offset:10240\n"
        "ds_read_b64_tr_b16 v[206:207], v5 offset:10752\n"
        "s_waitcnt lgkmcnt(12)\n"
        ".L10_%=:\n"
        "s_cmp_ge_u32 s20, 4\n"
        "s_cbranch_scc0 .L11_%=\n"
        "v_mfma_f32_32x32x16_bf16 v[96:111], v[48:51], v[48:51], 0\n"
        "v_and_b32 v55, 3, v0\n"
        "v_mul_u32_u24 v59, 200, v55\n"
        "v_and_b32 v56, 32, v0\n"
        "v_lshrrev_b32 v56, 1, v56\n"
        "s_mov_b32 s23, 162960\n"
        "v_add3_u32 v59, v59, v56, s23\n"
        "s_waitcnt lgkmcnt(0)\n"
        ".L11_%=:\n"
        "s_barrier\n"
        "ds_read_b128 v[16:19], v4\n"
        "ds_read_b128 v[20:23], v4 offset:1024\n"
        "s_cmp_eq_u32 s20, 3\n"
        "s_cbranch_scc0 .L12_%=\n"
        "s_add_u32 m0, s37, 8192\n"
        "s_add_u32 s23, s36, 54272\n"
        "buffer_load_dwordx4 v7, s[28:31], s23 offen offset:1024 lds\n"
        "s_add_u32 s23, s36, 59392\n"
        "buffer_load_dwordx4 v7, s[28:31], s23 offen offset:2048 lds\n"
        "s_add_u32 s23, s36, 64512\n"
        "buffer_load_dwordx4 v7, s[28:31], s23 offen offset:3072 lds\n"
        ".L12_%=:\n"
        "s_add_u32 s50, s49, s51\n"
        "s_bitcmp1_b32 s50, 11\n"
        "s_cselect_b32 s23, 5888, 0\n"
        "s_sub_u32 s45, s50, s23\n"
        "v_add_u32_e64 v57, s45, v8\n"
        "s_lshl_b32 s23, s23, 1\n"
        "s_add_u32 s23, s23, 256\n"
        "v_add_u32 v58, s23, v57\n"
        "s_mov_b32 exec_lo, 0x77ff77ff\n"
        "s_mov_b32 exec_hi, 0x77ff77ff\n"
        "s_waitcnt lgkmcnt(0)\n"
        "v_pk_fma_f32 v[12:13], v[12:13], v[16:17], v[20:21]\n"
        "v_pk_fma_f32 v[240:241], v[240:241], v[16:17], v[20:21]\n"
        "v_pk_fma_f32 v[224:225], v[224:225], v[16:17], v[20:21]\n"
        "v_pk_fma_f32 v[208:209], v[208:209], v[16:17], v[20:21]\n"
        "v_cvt_pk_bf16_f32 v24, v12, v240\n"
        "v_cvt_pk_bf16_f32 v25, v224, v208\n"
        "v_cvt_pk_bf16_f32 v26, v13, v241\n"
        "v_cvt_pk_bf16_f32 v27, v225, v209\n"
        "ds_write_b64 v1, v[24:25] offset:0\n"
        "ds_write_b64 v1, v[26:27] offset:200\n"
        "v_pk_fma_f32 v[244:245], v[244:245], v[16:17], v[20:21]\n"
        "v_pk_fma_f32 v[228:229], v[228:229], v[16:17], v[20:21]\n"
        "v_pk_fma_f32 v[212:213], v[212:213], v[16:17], v[20:21]\n"
        "v_pk_fma_f32 v[248:249], v[248:249], v[16:17], v[20:21]\n"
        "v_cvt_pk_bf16_f32 v28, v244, v228\n"
        "v_cvt_pk_bf16_f32 v29, v212, v248\n"
        "v_cvt_pk_bf16_f32 v30, v245, v229\n"
        "v_cvt_pk_bf16_f32 v31, v213, v249\n"
        "ds_write_b64 v1, v[28:29] offset:8\n"
        "ds_write_b64 v1, v[30:31] offset:208\n"
        "v_pk_fma_f32 v[232:233], v[232:233], v[16:17], v[20:21]\n"
        "v_pk_fma_f32 v[216:217], v[216:217], v[16:17], v[20:21]\n"
        "v_pk_fma_f32 v[252:253], v[252:253], v[16:17], v[20:21]\n"
        "v_pk_fma_f32 v[236:237], v[236:237], v[16:17], v[20:21]\n"
        "v_cvt_pk_bf16_f32 v32, v232, v216\n"
        "v_cvt_pk_bf16_f32 v33, v252, v236\n"
        "v_cvt_pk_bf16_f32 v34, v233, v217\n"
        "v_cvt_pk_bf16_f32 v35, v253, v237\n"
        "ds_write_b64 v1, v[32:33] offset:16\n"
        "ds_write_b64 v1, v[34:35] offset:216\n"
        "s_mov_b32 exec_lo, 0x43d277ff\n"
        "s_mov_b32 exec_hi, 0x43d277ff\n"
        "v_pk_fma_f32 v[14:15], v[14:15], v[18:19], v[22:23]\n"
        "v_pk_fma_f32 v[242:243], v[242:243], v[18:19], v[22:23]\n"
        "v_pk_fma_f32 v[226:227], v[226:227], v[18:19], v[22:23]\n"
        "v_pk_fma_f32 v[210:211], v[210:211], v[18:19], v[22:23]\n"
        "v_cvt_pk_bf16_f32 v36, v14, v242\n"
        "v_cvt_pk_bf16_f32 v37, v226, v210\n"
        "v_cvt_pk_bf16_f32 v38, v15, v243\n"
        "v_cvt_pk_bf16_f32 v39, v227, v211\n"
        "ds_write_b64 v1, v[36:37] offset:400\n"
        "ds_write_b64 v1, v[38:39] offset:600\n"
        "v_pk_fma_f32 v[246:247], v[246:247], v[18:19], v[22:23]\n"
        "v_pk_fma_f32 v[230:231], v[230:231], v[18:19], v[22:23]\n"
        "v_pk_fma_f32 v[214:215], v[214:215], v[18:19], v[22:23]\n"
        "v_pk_fma_f32 v[250:251], v[250:251], v[18:19], v[22:23]\n"
        "v_cvt_pk_bf16_f32 v40, v246, v230\n"
        "v_cvt_pk_bf16_f32 v41, v214, v250\n"
        "v_cvt_pk_bf16_f32 v42, v247, v231\n"
        "v_cvt_pk_bf16_f32 v43, v215, v251\n"
        "ds_write_b64 v1, v[40:41] offset:408\n"
        "ds_write_b64 v1, v[42:43] offset:608\n"
        "v_pk_fma_f32 v[234:235], v[234:235], v[18:19], v[22:23]\n"
        "v_pk_fma_f32 v[218:219], v[218:219], v[18:19], v[22:23]\n"
        "v_pk_fma_f32 v[254:255], v[254:255], v[18:19], v[22:23]\n"
        "v_pk_fma_f32 v[238:239], v[238:239], v[18:19], v[22:23]\n"
        "v_cvt_pk_bf16_f32 v44, v234, v218\n"
        "v_cvt_pk_bf16_f32 v45, v254, v238\n"
        "v_cvt_pk_bf16_f32 v46, v235, v219\n"
        "v_cvt_pk_bf16_f32 v47, v255, v239\n"
        "ds_write_b64 v1, v[44:45] offset:416\n"
        "ds_write_b64 v1, v[46:47] offset:616\n"
        "s_mov_b64 exec, -1\n"
        "s_waitcnt vmcnt(0)\n"
        "s_waitcnt lgkmcnt(0)\n"
        "s_barrier\n"
        "s_cmp_lt_u32 s20, 4\n"
        "s_cbranch_scc0 .L14_%=\n"
        "ds_read_b64 v[16:17], v10 offset:0\n"
        "ds_read_b64 v[18:19], v10 offset:8\n"
        "ds_read_b64 v[20:21], v10 offset:32\n"
        "ds_read_b64 v[22:23], v10 offset:40\n"
        "ds_read_b64 v[24:25], v10 offset:64\n"
        "ds_read_b64 v[26:27], v10 offset:72\n"
        "ds_read_b64 v[28:29], v10 offset:96\n"
        "ds_read_b64 v[30:31], v10 offset:104\n"
        "ds_read_b64 v[32:33], v10 offset:128\n"
        "ds_read_b64 v[34:35], v10 offset:136\n"
        "ds_read_b64 v[36:37], v10 offset:160\n"
        "ds_read_b64 v[38:39], v10 offset:168\n"
        "s_mov_b64 exec, 1\n"
        "ds_add_u32 v52, v53 offset:0\n"
        "s_mov_b64 exec, -1\n"
        "s_setprio 1\n"
        "s_mov_b32 s52, 2\n"
        "s_mov_b32 s51, 18432\n"
        "s_mov_b32 s60, 12544\n"
        "s_mov_b32 s61, 24320\n"
        "v_mov_b32 v7, 163780\n"
        "v_mov_b32_e64 v4, 0\n"
        "v_add_u32 v6, -18432, v9\n"
        "s_waitcnt lgkmcnt(1)\n"
        ".L14_%=:\n"
        "s_cmp_ge_u32 s20, 4\n"
        "s_cbranch_scc0 .L15_%=\n"
        "s_mov_b32 s4, s14\n"
        "s_and_b32 s5, s15, 0xffff\n"
        "s_mov_b32 s6, 73728\n"
        "s_mov_b32 s7, 0x20000\n"
        "s_sub_u32 s46, s20, 4\n"
        "s_mul_i32 s53, s46, 5120\n"
        "s_add_u32 m0, s53, 73728\n"
        "s_nop 0\n"
        "buffer_load_dwordx4 v2, s[4:7], s53 offen offset:0 lds\n"
        "buffer_load_dwordx4 v2, s[4:7], s53 offen offset:1024 lds\n"
        "buffer_load_dwordx4 v2, s[4:7], s53 offen offset:2048 lds\n"
        "buffer_load_dwordx4 v2, s[4:7], s53 offen offset:3072 lds\n"
        "s_add_u32 s53, s53, 4096\n"
        "s_add_u32 m0, s53, 73728\n"
        "s_nop 0\n"
        "s_nop 0\n"
        "buffer_load_dwordx4 v2, s[4:7], s53 offen offset:0 lds\n"
        "s_add_u32 s53, s53, 1024\n"
        "ds_read_b64 v[16:17], v10 offset:0\n"
        "ds_read_b64 v[18:19], v10 offset:8\n"
        "ds_read_b64 v[20:21], v10 offset:32\n"
        "ds_read_b64 v[22:23], v10 offset:40\n"
        "ds_read_b64 v[24:25], v10 offset:64\n"
        "ds_read_b64 v[26:27], v10 offset:72\n"
        "ds_read_b64 v[28:29], v10 offset:96\n"
        "ds_read_b64 v[30:31], v10 offset:104\n"
        "ds_read_b64 v[32:33], v10 offset:128\n"
        "ds_read_b64 v[34:35], v10 offset:136\n"
        "ds_read_b64 v[36:37], v10 offset:160\n"
        "ds_read_b64 v[38:39], v10 offset:168\n"
        "s_mov_b32 exec_lo, 0xf0000f\n"
        "s_mov_b32 exec_hi, 0xf0000f\n"
        "ds_read_b64 v[64:65], v59 offset:0\n"
        "ds_read_b64 v[66:67], v59 offset:8\n"
        "ds_read_b64 v[68:69], v59 offset:32\n"
        "ds_read_b64 v[70:71], v59 offset:40\n"
        "ds_read_b64 v[72:73], v59 offset:64\n"
        "ds_read_b64 v[74:75], v59 offset:72\n"
        "ds_read_b64 v[76:77], v59 offset:96\n"
        "ds_read_b64 v[78:79], v59 offset:104\n"
        "ds_read_b64 v[80:81], v59 offset:128\n"
        "ds_read_b64 v[82:83], v59 offset:136\n"
        "ds_read_b64 v[84:85], v59 offset:160\n"
        "ds_read_b64 v[86:87], v59 offset:168\n"
        "s_mov_b32 exec_lo, 0xf0000f00\n"
        "s_mov_b32 exec_hi, 0xf0000f00\n"
        "ds_read_b64 v[88:89], v59 offset:0\n"
        "ds_read_b64 v[90:91], v59 offset:8\n"
        "ds_read_b64 v[92:93], v59 offset:32\n"
        "ds_read_b64 v[94:95], v59 offset:40\n"
        "ds_read_b64 v[96:97], v59 offset:64\n"
        "ds_read_b64 v[98:99], v59 offset:72\n"
        "ds_read_b64 v[100:101], v59 offset:96\n"
        "ds_read_b64 v[102:103], v59 offset:104\n"
        "ds_read_b64 v[104:105], v59 offset:128\n"
        "ds_read_b64 v[106:107], v59 offset:136\n"
        "ds_read_b64 v[108:109], v59 offset:160\n"
        "ds_read_b64 v[110:111], v59 offset:168\n"
        "s_mov_b64 exec, 1\n"
        "ds_add_u32 v52, v53 offset:0\n"
        "s_mov_b64 exec, -1\n"
        "ds_read_b64_tr_b16 v[112:113], v57 offset:0\n"
        "ds_read_b64_tr_b16 v[114:115], v57 offset:512\n"
        "ds_read_b64_tr_b16 v[116:117], v57 offset:2048\n"
        "ds_read_b64_tr_b16 v[118:119], v57 offset:2560\n"
        "ds_read_b64_tr_b16 v[120:121], v57 offset:4096\n"
        "ds_read_b64_tr_b16 v[122:123], v57 offset:4608\n"
        "ds_read_b64_tr_b16 v[124:125], v57 offset:6144\n"
        "ds_read_b64_tr_b16 v[126:127], v57 offset:6656\n"
        "ds_read_b64_tr_b16 v[128:129], v57 offset:8192\n"
        "ds_read_b64_tr_b16 v[130:131], v57 offset:8704\n"
        "ds_read_b64_tr_b16 v[132:133], v57 offset:10240\n"
        "ds_read_b64_tr_b16 v[134:135], v57 offset:10752\n"
        "ds_read_b64_tr_b16 v[136:137], v58 offset:0\n"
        "ds_read_b64_tr_b16 v[138:139], v58 offset:512\n"
        "ds_read_b64_tr_b16 v[140:141], v58 offset:2048\n"
        "ds_read_b64_tr_b16 v[142:143], v58 offset:2560\n"
        "ds_read_b64_tr_b16 v[144:145], v58 offset:4096\n"
        "ds_read_b64_tr_b16 v[146:147], v58 offset:4608\n"
        "ds_read_b64_tr_b16 v[148:149], v58 offset:6144\n"
        "ds_read_b64_tr_b16 v[150:151], v58 offset:6656\n"
        "ds_read_b64_tr_b16 v[152:153], v58 offset:8192\n"
        "ds_read_b64_tr_b16 v[154:155], v58 offset:8704\n"
        "ds_read_b64_tr_b16 v[156:157], v58 offset:10240\n"
        "ds_read_b64_tr_b16 v[158:159], v58 offset:10752\n"
        "s_waitcnt vmcnt(0)\n"
        "s_mov_b64 exec, 1\n"
        "ds_add_u32 v52, v53 offset:4\n"
        "s_mov_b64 exec, -1\n"
        "s_waitcnt lgkmcnt(13)\n"
        "v_mfma_f32_16x16x32_bf16 v[208:211], v[112:115], v[64:67], 0\n"
        "v_mfma_f32_16x16x32_bf16 v[208:211], v[116:119], v[68:71], v[208:211]\n"
        "v_mfma_f32_16x16x32_bf16 v[208:211], v[120:123], v[72:75], v[208:211]\n"
        "v_mfma_f32_16x16x32_bf16 v[208:211], v[124:127], v[76:79], v[208:211]\n"
        "v_mfma_f32_16x16x32_bf16 v[208:211], v[128:131], v[80:83], v[208:211]\n"
        "v_mfma_f32_16x16x32_bf16 v[208:211], v[132:135], v[84:87], v[208:211]\n"
        "ds_read_b64_tr_b16 v[160:161], v57 offset:12288\n"
        "ds_read_b64_tr_b16 v[162:163], v57 offset:12800\n"
        "ds_read_b64_tr_b16 v[164:165], v57 offset:14336\n"
        "ds_read_b64_tr_b16 v[166:167], v57 offset:14848\n"
        "ds_read_b64_tr_b16 v[168:169], v57 offset:16384\n"
        "ds_read_b64_tr_b16 v[170:171], v57 offset:16896\n"
        "ds_read_b64_tr_b16 v[172:173], v57 offset:18432\n"
        "ds_read_b64_tr_b16 v[174:175], v57 offset:18944\n"
        "ds_read_b64_tr_b16 v[176:177], v57 offset:20480\n"
        "ds_read_b64_tr_b16 v[178:179], v57 offset:20992\n"
        "ds_read_b64_tr_b16 v[180:181], v57 offset:22528\n"
        "ds_read_b64_tr_b16 v[182:183], v57 offset:23040\n"
        "s_nop 0\n"
        "s_waitcnt lgkmcnt(12)\n"
        "v_mfma_f32_16x16x32_bf16 v[208:211], v[136:139], v[88:91], v[208:211]\n"
        "v_mfma_f32_16x16x32_bf16 v[208:211], v[140:143], v[92:95], v[208:211]\n"
        "v_mfma_f32_16x16x32_bf16 v[208:211], v[144:147], v[96:99], v[208:211]\n"
        "v_mfma_f32_16x16x32_bf16 v[208:211], v[148:151], v[100:103], v[208:211]\n"
        "v_mfma_f32_16x16x32_bf16 v[208:211], v[152:155], v[104:107], v[208:211]\n"
        "v_mfma_f32_16x16x32_bf16 v[208:211], v[156:159], v[108:111], v[208:211]\n"
        "ds_read_b64_tr_b16 v[184:185], v5 offset:0\n"
        "ds_read_b64_tr_b16 v[186:187], v5 offset:512\n"
        "ds_read_b64_tr_b16 v[188:189], v5 offset:2048\n"
        "ds_read_b64_tr_b16 v[190:191], v5 offset:2560\n"
        "ds_read_b64_tr_b16 v[192:193], v5 offset:4096\n"
        "ds_read_b64_tr_b16 v[194:195], v5 offset:4608\n"
        "ds_read_b64_tr_b16 v[196:197], v5 offset:6144\n"
        "ds_read_b64_tr_b16 v[198:199], v5 offset:6656\n"
        "ds_read_b64_tr_b16 v[200:201], v5 offset:8192\n"
        "ds_read_b64_tr_b16 v[202:203], v5 offset:8704\n"
        "ds_read_b64_tr_b16 v[204:205], v5 offset:10240\n"
        "ds_read_b64_tr_b16 v[206:207], v5 offset:10752\n"
        "ds_read_b32 v54, v52 offset:0\n"
        "s_waitcnt lgkmcnt(13)\n"
        "v_mfma_f32_16x16x32_bf16 v[212:215], v[160:163], v[64:67], 0\n"
        "s_mul_i32 s53, s46, 13312\n"
        "s_add_u32 s53, s53, 20480\n"
        "v_mfma_f32_16x16x32_bf16 v[212:215], v[164:167], v[68:71], v[212:215]\n"
        "s_mov_b32 s52, 0\n"
        "s_mov_b32 s60, s51\n"
        "v_mfma_f32_16x16x32_bf16 v[212:215], v[168:171], v[72:75], v[212:215]\n"
        "s_mov_b32 s61, s51\n"
        "s_sub_u32 s45, s49, s51\n"
        "v_mfma_f32_16x16x32_bf16 v[212:215], v[172:175], v[76:79], v[212:215]\n"
        "v_add_u32 v6, s45, v9\n"
        "v_mov_b32 v7, 163784\n"
        "v_mfma_f32_16x16x32_bf16 v[212:215], v[176:179], v[80:83], v[212:215]\n"
        "v_mov_b32_e64 v4, 1\n"
        "s_mul_i32 s59, s54, 6144\n"
        "v_mfma_f32_16x16x32_bf16 v[212:215], v[180:183], v[84:87], v[212:215]\n"
        "s_add_u32 s59, s59, 122880\n"
        "s_mul_i32 s46, s46, 768\n"
        "s_add_u32 s46, s46, 159744\n"
        "v_lshlrev_b32 v55, 5, v55\n"
        "v_and_b32 v12, 12, v0\n"
        "v_lshl_add_u32 v12, v12, 5, v55\n"
        "v_and_b32_e64 v13, 16, v0\n"
        "v_add3_u32 v12, v12, v13, s46\n"
        "v_bfe_u32 v13, v0, 5, 1\n"
        "v_lshl_add_u32 v12, v13, 3, v12\n"
        "v_lshl_add_u32 v10, v13, 4, v55\n"
        "v_add_u32_e64 v10, s46, v10\n"
        "v_add_u32 v11, s59, v2\n"
        "s_waitcnt lgkmcnt(0)\n"
        "v_readfirstlane_b32 s23, v54\n"
        "s_cmp_ge_u32 s23, 8\n"
        "s_cbranch_scc0 .L16_%=\n"
        ".L17_%=:\n"
        "s_add_u32 m0, s53, 73728\n"
        "s_nop 0\n"
        "buffer_load_dwordx4 v2, s[4:7], s53 offen offset:0 lds\n"
        "buffer_load_dwordx4 v2, s[4:7], s53 offen offset:1024 lds\n"
        "buffer_load_dwordx4 v2, s[4:7], s53 offen offset:2048 lds\n"
        "buffer_load_dwordx4 v2, s[4:7], s53 offen offset:3072 lds\n"
        "s_add_u32 s53, s53, 4096\n"
        "v_fmaak_f32 v40, v208, v208, 0x409d30fc\n"
        "v_fmaak_f32 v41, v209, v209, 0x409d30fc\n"
        "v_fmaak_f32 v42, v210, v210, 0x409d30fc\n"
        "v_fmaak_f32 v43, v211, v211, 0x409d30fc\n"
        "v_fmaak_f32 v44, v212, v212, 0x409d30fc\n"
        "v_fmaak_f32 v45, v213, v213, 0x409d30fc\n"
        "v_fmaak_f32 v46, v214, v214, 0x409d30fc\n"
        "v_fmaak_f32 v47, v215, v215, 0x409d30fc\n"
        "v_mul_f32 v40, v40, v208\n"
        "v_mul_f32 v41, v41, v209\n"
        "v_mul_f32 v42, v42, v210\n"
        "v_mul_f32 v43, v43, v211\n"
        "v_mul_f32 v44, v44, v212\n"
        "v_mul_f32 v45, v45, v213\n"
        "v_mul_f32_e64 v46, v46, v214\n"
        "v_mul_f32 v47, v47, v215\n"
        "v_exp_f32_e64 v40, -v40\n"
        "v_exp_f32_e64 v41, -v41\n"
        "v_exp_f32_e64 v42, -v42\n"
        "v_exp_f32_e64 v43, -v43\n"
        "v_exp_f32_e64 v44, -v44\n"
        "v_exp_f32_e64 v45, -v45\n"
        "v_exp_f32_e64 v46, -v46\n"
        "v_exp_f32_e64 v47, -v47\n"
        "s_add_u32 m0, s53, 73728\n"
        "s_nop 0\n"
        "buffer_load_dwordx4 v2, s[4:7], s53 offen offset:0 lds\n"
        "buffer_load_dwordx4 v2, s[4:7], s53 offen offset:1024 lds\n"
        "buffer_load_dwordx4 v2, s[4:7], s53 offen offset:2048 lds\n"
        "buffer_load_dwordx4 v2, s[4:7], s53 offen offset:3072 lds\n"
        "s_add_u32 s53, s53, 4096\n"
        "v_fma_f32 v40, v40, s56, s56\n"
        "v_fma_f32 v41, v41, s56, s56\n"
        "v_fma_f32 v42, v42, s56, s56\n"
        "v_fma_f32 v43, v43, s56, s56\n"
        "v_fma_f32 v44, v44, s56, s56\n"
        "v_fma_f32 v45, v45, s56, s56\n"
        "v_fma_f32 v46, v46, s56, s56\n"
        "v_fma_f32 v47, v47, s56, s56\n"
        "v_rcp_f32 v40, v40\n"
        "v_rcp_f32 v41, v41\n"
        "v_rcp_f32 v42, v42\n"
        "v_rcp_f32 v43, v43\n"
        "v_rcp_f32 v44, v44\n"
        "v_rcp_f32 v45, v45\n"
        "v_rcp_f32_e64 v46, v46\n"
        "v_rcp_f32 v47, v47\n"
        "s_add_u32 m0, s53, 73728\n"
        "s_nop 0\n"
        "buffer_load_dwordx4 v2, s[4:7], s53 offen offset:0 lds\n"
        "buffer_load_dwordx4 v2, s[4:7], s53 offen offset:1024 lds\n"
        "buffer_load_dwordx4 v2, s[4:7], s53 offen offset:2048 lds\n"
        "buffer_load_dwordx4 v2, s[4:7], s53 offen offset:3072 lds\n"
        "s_add_u32 s53, s53, 4096\n"
        "v_mul_f32 v40, v40, v208\n"
        "v_mul_f32 v41, v41, v209\n"
        "v_mul_f32 v42, v42, v210\n"
        "v_mul_f32 v43, v43, v211\n"
        "v_mul_f32 v44, v44, v212\n"
        "v_mul_f32 v45, v45, v213\n"
        "v_mul_f32 v46, v46, v214\n"
        "v_mul_f32 v47, v47, v215\n"
        "v_cvt_pk_bf16_f32 v208, v40, v41\n"
        "v_cvt_pk_bf16_f32 v209, v42, v43\n"
        "v_cvt_pk_bf16_f32 v210, v44, v45\n"
        "v_cvt_pk_bf16_f32 v211, v46, v47\n"
        "ds_write_b64 v12, v[208:209]\n"
        "s_mov_b32 exec_lo, 0xff00ff\n"
        "s_mov_b32 exec_hi, 0xff00ff\n"
        "ds_write_b64 v12, v[210:211] offset:512\n"
        "s_mov_b64 exec, -1\n"
        "s_add_u32 m0, s53, 73728\n"
        "s_nop 0\n"
        "buffer_load_dwordx4 v2, s[4:7], s53 offen offset:0 lds\n"
        "s_add_u32 s53, s53, 1024\n"
        "s_setprio 2\n"
        ".L15_%=:\n"
        "v_mfma_f32_32x32x16_bf16 v[112:127], v[184:187], v[16:19], 0\n"
        "s_mul_i32 s44, s21, 224\n"
        "s_add_u32 s44, s44, s22\n"
        "s_lshl_b32 s45, s54, 5\n"
        "s_add_u32 s23, s44, s45\n"
        "s_mul_i32 s23, s23, 192\n"
        "s_mov_b32 s46, 40320\n"
        "s_mov_b32 s36, s8\n"
        "s_and_b32 s37, s9, 0xffff\n"
        "s_mov_b32 s38, 9633792\n"
        "s_mov_b32 s39, 0x20000\n"
        "s_mov_b32 s40, s16\n"
        "v_mfma_f32_32x32x16_bf16 v[112:127], v[188:191], v[20:23], v[112:127]\n"
        "s_and_b32 s41, s17, 0xffff\n"
        "s_mov_b32 s42, 9633792\n"
        "s_mov_b32 s43, 0x20000\n"
        "v_and_b32_e64 v55, 31, v0\n"
        "v_mul_u32_u24 v14, 192, v55\n"
        "v_add_u32_e64 v55, s45, v55\n"
        "v_mul_u32_u24 v55, 4682, v55\n"
        "v_lshrrev_b32_e64 v55, 16, v55\n"
        "v_mad_u32_u24 v14, v55, s46, v14\n"
        "v_and_b32_e64 v55, 32, v0\n"
        "v_add3_u32 v14, v14, v55, s23\n"
        "v_mfma_f32_32x32x16_bf16 v[112:127], v[192:195], v[24:27], v[112:127]\n"
        "v_and_b32_e64 v55, 15, v0\n"
        "v_mul_u32_u24 v13, 192, v55\n"
        "v_and_b32_e64 v12, 48, v0\n"
        "v_add3_u32 v13, v13, v12, s23\n"
        "v_add_u32_e64 v55, s45, v55\n"
        "v_mul_u32_u24 v12, 4682, v55\n"
        "v_lshrrev_b32_e64 v12, 16, v12\n"
        "v_mad_u32_u24 v1, v12, s46, v13\n"
        "v_add_u32_e64 v55, 16, v55\n"
        "v_mul_u32_u24 v12, 4682, v55\n"
        "v_lshrrev_b32_e64 v12, 16, v12\n"
        "v_mfma_f32_32x32x16_bf16 v[112:127], v[196:199], v[28:31], v[112:127]\n"
        "v_mad_u32_u24 v15, v12, s46, v13\n"
        "v_add_u32 v15, 3072, v15\n"
        "v_and_b32 v55, 1, v0\n"
        "v_lshlrev_b32 v55, 4, v55\n"
        "v_mov_b32 v12, 0x3f80\n"
        "v_lshlrev_b32 v55, v55, v12\n"
        "s_mov_b32 s98, 0x3\n"
        "s_mov_b32 s99, 0x30\n"
        "v_cndmask_b32_e64 v56, 0, v55, s[98:99]\n"
        "s_mov_b32 s98, 0xc\n"
        "v_mfma_f32_32x32x16_bf16 v[112:127], v[200:203], v[32:35], v[112:127]\n"
        "s_mov_b32 s99, 0xc0\n"
        "v_cndmask_b32_e64 v57, 0, v55, s[98:99]\n"
        "s_mov_b32 s98, 0x300\n"
        "s_mov_b32 s99, 0x3000\n"
        "v_cndmask_b32_e64 v58, 0, v55, s[98:99]\n"
        "s_mov_b32 s98, 0xc00\n"
        "s_mov_b32 s99, 0xc000\n"
        "v_cndmask_b32_e64 v59, 0, v55, s[98:99]\n"
        "s_mov_b32 s98, 0x30000\n"
        "s_mov_b32 s99, 0x300000\n"
        "v_mfma_f32_32x32x16_bf16 v[112:127], v[204:207], v[36:39], v[112:127]\n"
        "v_cndmask_b32_e64 v60, 0, v55, s[98:99]\n"
        "s_mov_b32 s98, 0xc0000\n"
        "s_mov_b32 s99, 0xc00000\n"
        "v_cndmask_b32_e64 v61, 0, v55, s[98:99]\n"
        "s_mov_b32 s98, 0x3000000\n"
        "s_mov_b32 s99, 0x30000000\n"
        "v_cndmask_b32_e64 v62, 0, v55, s[98:99]\n"
        "s_mov_b32 s98, 0xc000000\n"
        "s_mov_b32 s99, 0xc0000000\n"
        "v_cndmask_b32_e64 v63, 0, v55, s[98:99]\n"
        "s_cmp_eq_u32 s55, 2\n"
        "s_cselect_b32 s38, 0, s42\n"
        "buffer_load_dwordx4 v[160:163], v14, s[36:39], 0 offen offset:0\n"
        "buffer_load_dwordx4 v[164:167], v14, s[36:39], 0 offen offset:64\n"
        "buffer_load_dwordx4 v[168:171], v14, s[36:39], 0 offen offset:128\n"
        "buffer_load_dwordx4 v[172:175], v14, s[36:39], 0 offen offset:16\n"
        "buffer_load_dwordx4 v[176:179], v14, s[36:39], 0 offen offset:80\n"
        "buffer_load_dwordx4 v[180:183], v14, s[36:39], 0 offen offset:144\n"
        "s_mov_b32 s38, s42\n"
        "ds_read_b64_tr_b16 v[184:185], v3 offset:0\n"
        "ds_read_b64_tr_b16 v[186:187], v3 offset:512\n"
        "ds_read_b64_tr_b16 v[188:189], v3 offset:2048\n"
        "ds_read_b64_tr_b16 v[190:191], v3 offset:2560\n"
        "ds_read_b64_tr_b16 v[192:193], v3 offset:4096\n"
        "ds_read_b64_tr_b16 v[194:195], v3 offset:4608\n"
        "ds_read_b64_tr_b16 v[196:197], v3 offset:6144\n"
        "ds_read_b64_tr_b16 v[198:199], v3 offset:6656\n"
        "ds_read_b64_tr_b16 v[200:201], v3 offset:8192\n"
        "ds_read_b64_tr_b16 v[202:203], v3 offset:8704\n"
        "ds_read_b64_tr_b16 v[204:205], v3 offset:10240\n"
        "ds_read_b64_tr_b16 v[206:207], v3 offset:10752\n"
        "v_pk_fma_f32 v[64:65], v[112:113], v[112:113], s[58:59] op_sel_hi:[1,1,0]\n"
        "v_pk_fma_f32 v[66:67], v[114:115], v[114:115], s[58:59] op_sel_hi:[1,1,0]\n"
        "v_pk_fma_f32 v[68:69], v[116:117], v[116:117], s[58:59] op_sel_hi:[1,1,0]\n"
        "v_pk_fma_f32 v[70:71], v[118:119], v[118:119], s[58:59] op_sel_hi:[1,1,0]\n"
        "v_pk_fma_f32 v[72:73], v[120:121], v[120:121], s[58:59] op_sel_hi:[1,1,0]\n"
        "v_pk_fma_f32 v[74:75], v[122:123], v[122:123], s[58:59] op_sel_hi:[1,1,0]\n"
        "v_pk_fma_f32 v[76:77], v[124:125], v[124:125], s[58:59] op_sel_hi:[1,1,0]\n"
        "v_pk_fma_f32 v[78:79], v[126:127], v[126:127], s[58:59] op_sel_hi:[1,1,0]\n"
        "v_pk_mul_f32 v[64:65], v[64:65], v[112:113] neg_lo:[0,1] neg_hi:[0,1]\n"
        "v_pk_mul_f32 v[66:67], v[66:67], v[114:115] neg_lo:[0,1] neg_hi:[0,1]\n"
        "v_pk_mul_f32 v[68:69], v[68:69], v[116:117] neg_lo:[0,1] neg_hi:[0,1]\n"
        "v_pk_mul_f32 v[70:71], v[70:71], v[118:119] neg_lo:[0,1] neg_hi:[0,1]\n"
        "v_pk_mul_f32 v[72:73], v[72:73], v[120:121] neg_lo:[0,1] neg_hi:[0,1]\n"
        "v_pk_mul_f32 v[74:75], v[74:75], v[122:123] neg_lo:[0,1] neg_hi:[0,1]\n"
        "v_pk_mul_f32 v[76:77], v[76:77], v[124:125] neg_lo:[0,1] neg_hi:[0,1]\n"
        "v_pk_mul_f32 v[78:79], v[78:79], v[126:127] neg_lo:[0,1] neg_hi:[0,1]\n"
        "s_waitcnt lgkmcnt(0)\n"
        "v_mfma_f32_32x32x16_bf16 v[128:143], v[184:187], v[16:19], 0\n"
        "v_exp_f32 v64, v64\n"
        "v_exp_f32 v65, v65\n"
        "v_exp_f32 v66, v66\n"
        "v_mfma_f32_32x32x16_bf16 v[128:143], v[188:191], v[20:23], v[128:143]\n"
        "v_exp_f32 v67, v67\n"
        "v_exp_f32 v68, v68\n"
        "v_exp_f32_e64 v69, v69\n"
        "v_mfma_f32_32x32x16_bf16 v[128:143], v[192:195], v[24:27], v[128:143]\n"
        "v_exp_f32 v70, v70\n"
        "v_exp_f32 v71, v71\n"
        "v_exp_f32_e64 v72, v72\n"
        "v_mfma_f32_32x32x16_bf16 v[128:143], v[196:199], v[28:31], v[128:143]\n"
        "v_exp_f32 v73, v73\n"
        "v_exp_f32 v74, v74\n"
        "v_exp_f32_e64 v75, v75\n"
        "v_mfma_f32_32x32x16_bf16 v[128:143], v[200:203], v[32:35], v[128:143]\n"
        "v_exp_f32 v76, v76\n"
        "v_exp_f32 v77, v77\n"
        "v_exp_f32_e64 v78, v78\n"
        "v_mfma_f32_32x32x16_bf16 v[128:143], v[204:207], v[36:39], v[128:143]\n"
        "v_exp_f32 v79, v79\n"
        "s_waitcnt vmcnt(6)\n"
        "s_mov_b64 exec, 1\n"
        "ds_add_u32 v52, v4 offset:8\n"
        "s_mov_b64 exec, -1\n"
        "ds_read_b64_tr_b16 v[184:185], v5 offset:12288\n"
        "ds_read_b64_tr_b16 v[186:187], v5 offset:12800\n"
        "ds_read_b64_tr_b16 v[188:189], v5 offset:14336\n"
        "ds_read_b64_tr_b16 v[190:191], v5 offset:14848\n"
        "ds_read_b64_tr_b16 v[192:193], v5 offset:16384\n"
        "ds_read_b64_tr_b16 v[194:195], v5 offset:16896\n"
        "ds_read_b64_tr_b16 v[196:197], v5 offset:18432\n"
        "ds_read_b64_tr_b16 v[198:199], v5 offset:18944\n"
        "ds_read_b64_tr_b16 v[200:201], v5 offset:20480\n"
        "ds_read_b64_tr_b16 v[202:203], v5 offset:20992\n"
        "ds_read_b64_tr_b16 v[204:205], v5 offset:22528\n"
        "ds_read_b64_tr_b16 v[206:207], v5 offset:23040\n"
        "ds_read_b32 v54, v7\n"
        "v_pk_fma_f32 v[64:65], v[64:65], s[56:57], s[56:57] op_sel_hi:[1,0,0]\n"
        "v_pk_fma_f32 v[66:67], v[66:67], s[56:57], s[56:57] op_sel_hi:[1,0,0]\n"
        "v_pk_fma_f32 v[68:69], v[68:69], s[56:57], s[56:57] op_sel_hi:[1,0,0]\n"
        "v_pk_fma_f32 v[70:71], v[70:71], s[56:57], s[56:57] op_sel_hi:[1,0,0]\n"
        "v_pk_fma_f32 v[72:73], v[72:73], s[56:57], s[56:57] op_sel_hi:[1,0,0]\n"
        "v_pk_fma_f32 v[74:75], v[74:75], s[56:57], s[56:57] op_sel_hi:[1,0,0]\n"
        "v_pk_fma_f32 v[76:77], v[76:77], s[56:57], s[56:57] op_sel_hi:[1,0,0]\n"
        "v_pk_fma_f32 v[78:79], v[78:79], s[56:57], s[56:57] op_sel_hi:[1,0,0]\n"
        "v_pk_fma_f32 v[80:81], v[128:129], v[128:129], s[58:59] op_sel_hi:[1,1,0]\n"
        "v_pk_fma_f32 v[82:83], v[130:131], v[130:131], s[58:59] op_sel_hi:[1,1,0]\n"
        "v_pk_fma_f32 v[84:85], v[132:133], v[132:133], s[58:59] op_sel_hi:[1,1,0]\n"
        "v_pk_fma_f32 v[86:87], v[134:135], v[134:135], s[58:59] op_sel_hi:[1,1,0]\n"
        "v_pk_fma_f32 v[88:89], v[136:137], v[136:137], s[58:59] op_sel_hi:[1,1,0]\n"
        "v_pk_fma_f32 v[90:91], v[138:139], v[138:139], s[58:59] op_sel_hi:[1,1,0]\n"
        "v_pk_fma_f32 v[92:93], v[140:141], v[140:141], s[58:59] op_sel_hi:[1,1,0]\n"
        "v_pk_fma_f32 v[94:95], v[142:143], v[142:143], s[58:59] op_sel_hi:[1,1,0]\n"
        "v_pk_mul_f32 v[80:81], v[80:81], v[128:129] neg_lo:[0,1] neg_hi:[0,1]\n"
        "v_pk_mul_f32 v[82:83], v[82:83], v[130:131] neg_lo:[0,1] neg_hi:[0,1]\n"
        "v_pk_mul_f32 v[84:85], v[84:85], v[132:133] neg_lo:[0,1] neg_hi:[0,1]\n"
        "v_pk_mul_f32 v[86:87], v[86:87], v[134:135] neg_lo:[0,1] neg_hi:[0,1]\n"
        "v_pk_mul_f32 v[88:89], v[88:89], v[136:137] neg_lo:[0,1] neg_hi:[0,1]\n"
        "v_pk_mul_f32 v[90:91], v[90:91], v[138:139] neg_lo:[0,1] neg_hi:[0,1]\n"
        "v_pk_mul_f32 v[92:93], v[92:93], v[140:141] neg_lo:[0,1] neg_hi:[0,1]\n"
        "v_pk_mul_f32 v[94:95], v[94:95], v[142:143] neg_lo:[0,1] neg_hi:[0,1]\n"
        "s_waitcnt lgkmcnt(0)\n"
        "v_mfma_f32_32x32x16_bf16 v[144:159], v[184:187], v[16:19], 0\n"
        "v_exp_f32 v80, v80\n"
        "v_exp_f32 v81, v81\n"
        "v_exp_f32 v82, v82\n"
        "v_mfma_f32_32x32x16_bf16 v[144:159], v[188:191], v[20:23], v[144:159]\n"
        "v_exp_f32 v83, v83\n"
        "v_exp_f32 v84, v84\n"
        "v_exp_f32_e64 v85, v85\n"
        "v_mfma_f32_32x32x16_bf16 v[144:159], v[192:195], v[24:27], v[144:159]\n"
        "v_exp_f32 v86, v86\n"
        "v_exp_f32 v87, v87\n"
        "v_exp_f32_e64 v88, v88\n"
        "v_mfma_f32_32x32x16_bf16 v[144:159], v[196:199], v[28:31], v[144:159]\n"
        "v_exp_f32 v89, v89\n"
        "v_exp_f32 v90, v90\n"
        "v_exp_f32_e64 v91, v91\n"
        "v_mfma_f32_32x32x16_bf16 v[144:159], v[200:203], v[32:35], v[144:159]\n"
        "v_exp_f32 v92, v92\n"
        "v_exp_f32 v93, v93\n"
        "v_exp_f32_e64 v94, v94\n"
        "v_mfma_f32_32x32x16_bf16 v[144:159], v[204:207], v[36:39], v[144:159]\n"
        "v_exp_f32 v95, v95\n"
        "v_rcp_f32 v64, v64\n"
        "v_rcp_f32 v65, v65\n"
        "s_waitcnt vmcnt(0)\n"
        "v_mfma_f32_32x32x16_bf16 v[208:223], v[56:59], v[160:163], 0\n"
        "v_rcp_f32 v66, v66\n"
        "v_rcp_f32 v67, v67\n"
        "v_rcp_f32_e64 v68, v68\n"
        "v_mfma_f32_32x32x16_bf16 v[224:239], v[56:59], v[164:167], 0\n"
        "v_rcp_f32 v69, v69\n"
        "v_rcp_f32 v70, v70\n"
        "v_rcp_f32_e64 v71, v71\n"
        "v_mfma_f32_32x32x16_bf16 v[240:255], v[56:59], v[168:171], 0\n"
        "v_rcp_f32 v72, v72\n"
        "v_rcp_f32 v73, v73\n"
        "v_rcp_f32_e64 v74, v74\n"
        "v_mfma_f32_32x32x16_bf16 v[208:223], v[60:63], v[172:175], v[208:223]\n"
        "v_rcp_f32 v75, v75\n"
        "v_rcp_f32 v76, v76\n"
        "v_rcp_f32_e64 v77, v77\n"
        "v_mfma_f32_32x32x16_bf16 v[224:239], v[60:63], v[176:179], v[224:239]\n"
        "v_rcp_f32 v78, v78\n"
        "v_rcp_f32 v79, v79\n"
        "v_mfma_f32_32x32x16_bf16 v[240:255], v[60:63], v[180:183], v[240:255]\n"
        "s_cmp_ge_u32 s20, 4\n"
        "s_cbranch_scc0 .L18_%=\n"
        "s_setprio 0\n"
        ".L18_%=:\n"
        "s_nop 0\n"
        "s_nop 0\n"
        "s_nop 0\n"
        "s_nop 0\n"
        "s_nop 0\n"
        "s_nop 0\n"
        "s_nop 0\n"
        "s_nop 0\n"
        ".L19_%=:\n"
        "v_readfirstlane_b32 s23, v54\n"
        "s_cmp_ge_u32 s23, 4\n"
        "s_cbranch_scc0 .L20_%=\n"
        ".L21_%=:\n"
        "v_add_u32 v5, s60, v5\n"
        "v_add_u32 v6, s51, v6\n"
        "ds_read_b64_tr_b16 v[184:185], v5 offset:0\n"
        "ds_read_b64_tr_b16 v[186:187], v5 offset:512\n"
        "ds_read_b64_tr_b16 v[188:189], v5 offset:2048\n"
        "ds_read_b64_tr_b16 v[190:191], v5 offset:2560\n"
        "ds_read_b64_tr_b16 v[192:193], v5 offset:4096\n"
        "ds_read_b64_tr_b16 v[194:195], v5 offset:4608\n"
        "ds_read_b64_tr_b16 v[196:197], v5 offset:6144\n"
        "ds_read_b64_tr_b16 v[198:199], v5 offset:6656\n"
        "ds_read_b64_tr_b16 v[200:201], v5 offset:8192\n"
        "ds_read_b64_tr_b16 v[202:203], v5 offset:8704\n"
        "ds_read_b64_tr_b16 v[204:205], v5 offset:10240\n"
        "ds_read_b64_tr_b16 v[206:207], v5 offset:10752\n"
        "ds_read_b64_tr_b16 v[160:161], v6 offset:0\n"
        "ds_read_b64_tr_b16 v[162:163], v6 offset:1536\n"
        "ds_read_b64_tr_b16 v[164:165], v6 offset:64\n"
        "ds_read_b64_tr_b16 v[166:167], v6 offset:1600\n"
        "ds_read_b64_tr_b16 v[168:169], v6 offset:128\n"
        "ds_read_b64_tr_b16 v[170:171], v6 offset:1664\n"
        "ds_read_b64_tr_b16 v[172:173], v6 offset:3072\n"
        "ds_read_b64_tr_b16 v[174:175], v6 offset:4608\n"
        "ds_read_b64_tr_b16 v[176:177], v6 offset:3136\n"
        "ds_read_b64_tr_b16 v[178:179], v6 offset:4672\n"
        "ds_read_b64_tr_b16 v[180:181], v6 offset:3200\n"
        "ds_read_b64_tr_b16 v[182:183], v6 offset:4736\n"
        "v_add_u32_e64 v3, s61, v3\n"
        "v_mov_b32 v7, 163784\n"
        "s_xor_b32 s60, s60, 28160\n"
        "s_xor_b32 s61, s61, 28160\n"
        "v_pk_fma_f32 v[80:81], v[80:81], s[56:57], s[56:57] op_sel_hi:[1,0,0]\n"
        "v_pk_fma_f32 v[82:83], v[82:83], s[56:57], s[56:57] op_sel_hi:[1,0,0]\n"
        "v_pk_fma_f32 v[84:85], v[84:85], s[56:57], s[56:57] op_sel_hi:[1,0,0]\n"
        "v_pk_fma_f32 v[86:87], v[86:87], s[56:57], s[56:57] op_sel_hi:[1,0,0]\n"
        "v_pk_fma_f32 v[88:89], v[88:89], s[56:57], s[56:57] op_sel_hi:[1,0,0]\n"
        "v_pk_fma_f32 v[90:91], v[90:91], s[56:57], s[56:57] op_sel_hi:[1,0,0]\n"
        "v_pk_fma_f32 v[92:93], v[92:93], s[56:57], s[56:57] op_sel_hi:[1,0,0]\n"
        "v_pk_fma_f32 v[94:95], v[94:95], s[56:57], s[56:57] op_sel_hi:[1,0,0]\n"
        "v_pk_mul_f32 v[64:65], v[64:65], v[112:113]\n"
        "v_pk_mul_f32 v[66:67], v[66:67], v[114:115]\n"
        "v_pk_mul_f32 v[68:69], v[68:69], v[116:117]\n"
        "v_pk_mul_f32 v[70:71], v[70:71], v[118:119]\n"
        "v_pk_mul_f32 v[72:73], v[72:73], v[120:121]\n"
        "v_pk_mul_f32 v[74:75], v[74:75], v[122:123]\n"
        "v_pk_mul_f32 v[76:77], v[76:77], v[124:125]\n"
        "v_pk_mul_f32 v[78:79], v[78:79], v[126:127]\n"
        "v_pk_fma_f32 v[96:97], v[144:145], v[144:145], s[58:59] op_sel_hi:[1,1,0]\n"
        "v_pk_fma_f32 v[98:99], v[146:147], v[146:147], s[58:59] op_sel_hi:[1,1,0]\n"
        "v_pk_fma_f32 v[100:101], v[148:149], v[148:149], s[58:59] op_sel_hi:[1,1,0]\n"
        "v_pk_fma_f32 v[102:103], v[150:151], v[150:151], s[58:59] op_sel_hi:[1,1,0]\n"
        "v_pk_fma_f32 v[104:105], v[152:153], v[152:153], s[58:59] op_sel_hi:[1,1,0]\n"
        "v_pk_fma_f32 v[106:107], v[154:155], v[154:155], s[58:59] op_sel_hi:[1,1,0]\n"
        "v_pk_fma_f32 v[108:109], v[156:157], v[156:157], s[58:59] op_sel_hi:[1,1,0]\n"
        "v_pk_fma_f32 v[110:111], v[158:159], v[158:159], s[58:59] op_sel_hi:[1,1,0]\n"
        "v_pk_mul_f32 v[96:97], v[96:97], v[144:145] neg_lo:[0,1] neg_hi:[0,1]\n"
        "v_pk_mul_f32 v[98:99], v[98:99], v[146:147] neg_lo:[0,1] neg_hi:[0,1]\n"
        "v_pk_mul_f32 v[100:101], v[100:101], v[148:149] neg_lo:[0,1] neg_hi:[0,1]\n"
        "v_pk_mul_f32 v[102:103], v[102:103], v[150:151] neg_lo:[0,1] neg_hi:[0,1]\n"
        "v_pk_mul_f32 v[104:105], v[104:105], v[152:153] neg_lo:[0,1] neg_hi:[0,1]\n"
        "v_pk_mul_f32 v[106:107], v[106:107], v[154:155] neg_lo:[0,1] neg_hi:[0,1]\n"
        "v_pk_mul_f32 v[108:109], v[108:109], v[156:157] neg_lo:[0,1] neg_hi:[0,1]\n"
        "v_pk_mul_f32 v[110:111], v[110:111], v[158:159] neg_lo:[0,1] neg_hi:[0,1]\n"
        "s_waitcnt lgkmcnt(0)\n"
        "v_mfma_f32_32x32x16_bf16 v[112:127], v[184:187], v[16:19], 0\n"
        "v_cvt_pk_bf16_f32 v40, v64, v65\n"
        "v_cvt_pk_bf16_f32 v41, v66, v67\n"
        "v_exp_f32 v96, v96\n"
        "v_exp_f32_e64 v97, v97\n"
        "v_mfma_f32_32x32x16_bf16 v[112:127], v[188:191], v[20:23], v[112:127]\n"
        "v_cvt_pk_bf16_f32 v42, v68, v69\n"
        "v_cvt_pk_bf16_f32 v43, v70, v71\n"
        "v_exp_f32 v98, v98\n"
        "v_exp_f32 v99, v99\n"
        "v_mfma_f32_32x32x16_bf16 v[112:127], v[192:195], v[24:27], v[112:127]\n"
        "v_cvt_pk_bf16_f32 v44, v72, v73\n"
        "v_cvt_pk_bf16_f32 v45, v74, v75\n"
        "v_exp_f32 v100, v100\n"
        "v_exp_f32 v101, v101\n"
        "v_mfma_f32_32x32x16_bf16 v[112:127], v[196:199], v[28:31], v[112:127]\n"
        "v_cvt_pk_bf16_f32 v46, v76, v77\n"
        "v_cvt_pk_bf16_f32 v47, v78, v79\n"
        "v_exp_f32 v102, v102\n"
        "v_exp_f32 v103, v103\n"
        "v_mfma_f32_32x32x16_bf16 v[112:127], v[200:203], v[32:35], v[112:127]\n"
        "v_exp_f32 v104, v104\n"
        "v_exp_f32 v105, v105\n"
        "v_exp_f32_e64 v106, v106\n"
        "v_mfma_f32_32x32x16_bf16 v[112:127], v[204:207], v[36:39], v[112:127]\n"
        "v_exp_f32 v107, v107\n"
        "v_exp_f32 v108, v108\n"
        "v_exp_f32_e64 v109, v109\n"
        "v_mfma_f32_32x32x16_bf16 v[208:223], v[160:163], v[40:43], v[208:223]\n"
        "v_exp_f32 v110, v110\n"
        "v_exp_f32 v111, v111\n"
        "v_rcp_f32_e64 v80, v80\n"
        "v_mfma_f32_32x32x16_bf16 v[208:223], v[172:175], v[44:47], v[208:223]\n"
        "v_rcp_f32 v81, v81\n"
        "v_rcp_f32 v82, v82\n"
        "v_rcp_f32_e64 v83, v83\n"
        "v_mfma_f32_32x32x16_bf16 v[224:239], v[164:167], v[40:43], v[224:239]\n"
        "v_rcp_f32 v84, v84\n"
        "v_rcp_f32 v85, v85\n"
        "v_rcp_f32_e64 v86, v86\n"
        "v_mfma_f32_32x32x16_bf16 v[224:239], v[176:179], v[44:47], v[224:239]\n"
        "v_rcp_f32 v87, v87\n"
        "v_rcp_f32 v88, v88\n"
        "v_rcp_f32_e64 v89, v89\n"
        "v_mfma_f32_32x32x16_bf16 v[240:255], v[168:171], v[40:43], v[240:255]\n"
        "v_rcp_f32 v90, v90\n"
        "v_rcp_f32 v91, v91\n"
        "v_rcp_f32_e64 v92, v92\n"
        "v_mfma_f32_32x32x16_bf16 v[240:255], v[180:183], v[44:47], v[240:255]\n"
        "v_rcp_f32 v93, v93\n"
        "v_rcp_f32 v94, v94\n"
        "v_rcp_f32_e64 v95, v95\n"
        "ds_read_b64_tr_b16 v[184:185], v3 offset:0\n"
        "ds_read_b64_tr_b16 v[186:187], v3 offset:512\n"
        "ds_read_b64_tr_b16 v[188:189], v3 offset:2048\n"
        "ds_read_b64_tr_b16 v[190:191], v3 offset:2560\n"
        "ds_read_b64_tr_b16 v[192:193], v3 offset:4096\n"
        "ds_read_b64_tr_b16 v[194:195], v3 offset:4608\n"
        "ds_read_b64_tr_b16 v[196:197], v3 offset:6144\n"
        "ds_read_b64_tr_b16 v[198:199], v3 offset:6656\n"
        "ds_read_b64_tr_b16 v[200:201], v3 offset:8192\n"
        "ds_read_b64_tr_b16 v[202:203], v3 offset:8704\n"
        "ds_read_b64_tr_b16 v[204:205], v3 offset:10240\n"
        "ds_read_b64_tr_b16 v[206:207], v3 offset:10752\n"
        "ds_read_b64_tr_b16 v[160:161], v6 offset:6144\n"
        "ds_read_b64_tr_b16 v[162:163], v6 offset:7680\n"
        "ds_read_b64_tr_b16 v[164:165], v6 offset:6208\n"
        "ds_read_b64_tr_b16 v[166:167], v6 offset:7744\n"
        "ds_read_b64_tr_b16 v[168:169], v6 offset:6272\n"
        "ds_read_b64_tr_b16 v[170:171], v6 offset:7808\n"
        "ds_read_b64_tr_b16 v[172:173], v6 offset:9216\n"
        "ds_read_b64_tr_b16 v[174:175], v6 offset:10752\n"
        "ds_read_b64_tr_b16 v[176:177], v6 offset:9280\n"
        "ds_read_b64_tr_b16 v[178:179], v6 offset:10816\n"
        "ds_read_b64_tr_b16 v[180:181], v6 offset:9344\n"
        "ds_read_b64_tr_b16 v[182:183], v6 offset:10880\n"
        "v_pk_fma_f32 v[96:97], v[96:97], s[56:57], s[56:57] op_sel_hi:[1,0,0]\n"
        "v_pk_fma_f32 v[98:99], v[98:99], s[56:57], s[56:57] op_sel_hi:[1,0,0]\n"
        "v_pk_fma_f32 v[100:101], v[100:101], s[56:57], s[56:57] op_sel_hi:[1,0,0]\n"
        "v_pk_fma_f32 v[102:103], v[102:103], s[56:57], s[56:57] op_sel_hi:[1,0,0]\n"
        "v_pk_fma_f32 v[104:105], v[104:105], s[56:57], s[56:57] op_sel_hi:[1,0,0]\n"
        "v_pk_fma_f32 v[106:107], v[106:107], s[56:57], s[56:57] op_sel_hi:[1,0,0]\n"
        "v_pk_fma_f32 v[108:109], v[108:109], s[56:57], s[56:57] op_sel_hi:[1,0,0]\n"
        "v_pk_fma_f32 v[110:111], v[110:111], s[56:57], s[56:57] op_sel_hi:[1,0,0]\n"
        "v_pk_mul_f32 v[80:81], v[80:81], v[128:129]\n"
        "v_pk_mul_f32 v[82:83], v[82:83], v[130:131]\n"
        "v_pk_mul_f32 v[84:85], v[84:85], v[132:133]\n"
        "v_pk_mul_f32 v[86:87], v[86:87], v[134:135]\n"
        "v_pk_mul_f32 v[88:89], v[88:89], v[136:137]\n"
        "v_pk_mul_f32 v[90:91], v[90:91], v[138:139]\n"
        "v_pk_mul_f32 v[92:93], v[92:93], v[140:141]\n"
        "v_pk_mul_f32 v[94:95], v[94:95], v[142:143]\n"
        "v_pk_fma_f32 v[64:65], v[112:113], v[112:113], s[58:59] op_sel_hi:[1,1,0]\n"
        "v_pk_fma_f32 v[66:67], v[114:115], v[114:115], s[58:59] op_sel_hi:[1,1,0]\n"
        "v_pk_fma_f32 v[68:69], v[116:117], v[116:117], s[58:59] op_sel_hi:[1,1,0]\n"
        "v_pk_fma_f32 v[70:71], v[118:119], v[118:119], s[58:59] op_sel_hi:[1,1,0]\n"
        "v_pk_fma_f32 v[72:73], v[120:121], v[120:121], s[58:59] op_sel_hi:[1,1,0]\n"
        "v_pk_fma_f32 v[74:75], v[122:123], v[122:123], s[58:59] op_sel_hi:[1,1,0]\n"
        "v_pk_fma_f32 v[76:77], v[124:125], v[124:125], s[58:59] op_sel_hi:[1,1,0]\n"
        "v_pk_fma_f32 v[78:79], v[126:127], v[126:127], s[58:59] op_sel_hi:[1,1,0]\n"
        "v_pk_mul_f32 v[64:65], v[64:65], v[112:113] neg_lo:[0,1] neg_hi:[0,1]\n"
        "v_pk_mul_f32 v[66:67], v[66:67], v[114:115] neg_lo:[0,1] neg_hi:[0,1]\n"
        "v_pk_mul_f32 v[68:69], v[68:69], v[116:117] neg_lo:[0,1] neg_hi:[0,1]\n"
        "v_pk_mul_f32 v[70:71], v[70:71], v[118:119] neg_lo:[0,1] neg_hi:[0,1]\n"
        "v_pk_mul_f32 v[72:73], v[72:73], v[120:121] neg_lo:[0,1] neg_hi:[0,1]\n"
        "v_pk_mul_f32 v[74:75], v[74:75], v[122:123] neg_lo:[0,1] neg_hi:[0,1]\n"
        "v_pk_mul_f32 v[76:77], v[76:77], v[124:125] neg_lo:[0,1] neg_hi:[0,1]\n"
        "v_pk_mul_f32 v[78:79], v[78:79], v[126:127] neg_lo:[0,1] neg_hi:[0,1]\n"
        "s_waitcnt lgkmcnt(0)\n"
        "v_mfma_f32_32x32x16_bf16 v[128:143], v[184:187], v[16:19], 0\n"
        "v_cvt_pk_bf16_f32 v40, v80, v81\n"
        "v_cvt_pk_bf16_f32 v41, v82, v83\n"
        "v_exp_f32 v64, v64\n"
        "v_exp_f32_e64 v65, v65\n"
        "v_mfma_f32_32x32x16_bf16 v[128:143], v[188:191], v[20:23], v[128:143]\n"
        "v_cvt_pk_bf16_f32 v42, v84, v85\n"
        "v_cvt_pk_bf16_f32 v43, v86, v87\n"
        "v_exp_f32 v66, v66\n"
        "v_exp_f32 v67, v67\n"
        "v_mfma_f32_32x32x16_bf16 v[128:143], v[192:195], v[24:27], v[128:143]\n"
        "v_cvt_pk_bf16_f32 v44, v88, v89\n"
        "v_cvt_pk_bf16_f32 v45, v90, v91\n"
        "v_exp_f32 v68, v68\n"
        "v_exp_f32 v69, v69\n"
        "v_mfma_f32_32x32x16_bf16 v[128:143], v[196:199], v[28:31], v[128:143]\n"
        "v_cvt_pk_bf16_f32 v46, v92, v93\n"
        "v_cvt_pk_bf16_f32 v47, v94, v95\n"
        "v_exp_f32 v70, v70\n"
        "v_exp_f32 v71, v71\n"
        "v_mfma_f32_32x32x16_bf16 v[128:143], v[200:203], v[32:35], v[128:143]\n"
        "v_exp_f32 v72, v72\n"
        "v_exp_f32 v73, v73\n"
        "v_exp_f32_e64 v74, v74\n"
        "v_mfma_f32_32x32x16_bf16 v[128:143], v[204:207], v[36:39], v[128:143]\n"
        "v_exp_f32 v75, v75\n"
        "v_exp_f32 v76, v76\n"
        "v_exp_f32_e64 v77, v77\n"
        "v_mfma_f32_32x32x16_bf16 v[208:223], v[160:163], v[40:43], v[208:223]\n"
        "v_exp_f32 v78, v78\n"
        "v_exp_f32 v79, v79\n"
        "v_rcp_f32_e64 v96, v96\n"
        "v_mfma_f32_32x32x16_bf16 v[208:223], v[172:175], v[44:47], v[208:223]\n"
        "v_rcp_f32 v97, v97\n"
        "v_rcp_f32 v98, v98\n"
        "v_rcp_f32_e64 v99, v99\n"
        "v_mfma_f32_32x32x16_bf16 v[224:239], v[164:167], v[40:43], v[224:239]\n"
        "v_rcp_f32 v100, v100\n"
        "v_rcp_f32 v101, v101\n"
        "v_rcp_f32_e64 v102, v102\n"
        "v_mfma_f32_32x32x16_bf16 v[224:239], v[176:179], v[44:47], v[224:239]\n"
        "v_rcp_f32 v103, v103\n"
        "v_rcp_f32 v104, v104\n"
        "v_rcp_f32_e64 v105, v105\n"
        "v_mfma_f32_32x32x16_bf16 v[240:255], v[168:171], v[40:43], v[240:255]\n"
        "v_rcp_f32 v106, v106\n"
        "v_rcp_f32 v107, v107\n"
        "v_rcp_f32_e64 v108, v108\n"
        "v_mfma_f32_32x32x16_bf16 v[240:255], v[180:183], v[44:47], v[240:255]\n"
        "v_rcp_f32 v109, v109\n"
        "v_rcp_f32 v110, v110\n"
        "v_rcp_f32_e64 v111, v111\n"
        "ds_read_b64_tr_b16 v[184:185], v5 offset:12288\n"
        "ds_read_b64_tr_b16 v[186:187], v5 offset:12800\n"
        "ds_read_b64_tr_b16 v[188:189], v5 offset:14336\n"
        "ds_read_b64_tr_b16 v[190:191], v5 offset:14848\n"
        "ds_read_b64_tr_b16 v[192:193], v5 offset:16384\n"
        "ds_read_b64_tr_b16 v[194:195], v5 offset:16896\n"
        "ds_read_b64_tr_b16 v[196:197], v5 offset:18432\n"
        "ds_read_b64_tr_b16 v[198:199], v5 offset:18944\n"
        "ds_read_b64_tr_b16 v[200:201], v5 offset:20480\n"
        "ds_read_b64_tr_b16 v[202:203], v5 offset:20992\n"
        "ds_read_b64_tr_b16 v[204:205], v5 offset:22528\n"
        "ds_read_b64_tr_b16 v[206:207], v5 offset:23040\n"
        "ds_read_b64_tr_b16 v[160:161], v6 offset:12288\n"
        "ds_read_b64_tr_b16 v[162:163], v6 offset:13824\n"
        "ds_read_b64_tr_b16 v[164:165], v6 offset:12352\n"
        "ds_read_b64_tr_b16 v[166:167], v6 offset:13888\n"
        "ds_read_b64_tr_b16 v[168:169], v6 offset:12416\n"
        "ds_read_b64_tr_b16 v[170:171], v6 offset:13952\n"
        "ds_read_b64_tr_b16 v[172:173], v6 offset:15360\n"
        "ds_read_b64_tr_b16 v[174:175], v6 offset:16896\n"
        "ds_read_b64_tr_b16 v[176:177], v6 offset:15424\n"
        "ds_read_b64_tr_b16 v[178:179], v6 offset:16960\n"
        "ds_read_b64_tr_b16 v[180:181], v6 offset:15488\n"
        "ds_read_b64_tr_b16 v[182:183], v6 offset:17024\n"
        "ds_read_b32 v54, v7\n"
        "v_pk_fma_f32 v[64:65], v[64:65], s[56:57], s[56:57] op_sel_hi:[1,0,0]\n"
        "v_pk_fma_f32 v[66:67], v[66:67], s[56:57], s[56:57] op_sel_hi:[1,0,0]\n"
        "v_pk_fma_f32 v[68:69], v[68:69], s[56:57], s[56:57] op_sel_hi:[1,0,0]\n"
        "v_pk_fma_f32 v[70:71], v[70:71], s[56:57], s[56:57] op_sel_hi:[1,0,0]\n"
        "v_pk_fma_f32 v[72:73], v[72:73], s[56:57], s[56:57] op_sel_hi:[1,0,0]\n"
        "v_pk_fma_f32 v[74:75], v[74:75], s[56:57], s[56:57] op_sel_hi:[1,0,0]\n"
        "v_pk_fma_f32 v[76:77], v[76:77], s[56:57], s[56:57] op_sel_hi:[1,0,0]\n"
        "v_pk_fma_f32 v[78:79], v[78:79], s[56:57], s[56:57] op_sel_hi:[1,0,0]\n"
        "v_pk_mul_f32 v[96:97], v[96:97], v[144:145]\n"
        "v_pk_mul_f32 v[98:99], v[98:99], v[146:147]\n"
        "v_pk_mul_f32 v[100:101], v[100:101], v[148:149]\n"
        "v_pk_mul_f32 v[102:103], v[102:103], v[150:151]\n"
        "v_pk_mul_f32 v[104:105], v[104:105], v[152:153]\n"
        "v_pk_mul_f32 v[106:107], v[106:107], v[154:155]\n"
        "v_pk_mul_f32 v[108:109], v[108:109], v[156:157]\n"
        "v_pk_mul_f32 v[110:111], v[110:111], v[158:159]\n"
        "v_pk_fma_f32 v[80:81], v[128:129], v[128:129], s[58:59] op_sel_hi:[1,1,0]\n"
        "v_pk_fma_f32 v[82:83], v[130:131], v[130:131], s[58:59] op_sel_hi:[1,1,0]\n"
        "v_pk_fma_f32 v[84:85], v[132:133], v[132:133], s[58:59] op_sel_hi:[1,1,0]\n"
        "v_pk_fma_f32 v[86:87], v[134:135], v[134:135], s[58:59] op_sel_hi:[1,1,0]\n"
        "v_pk_fma_f32 v[88:89], v[136:137], v[136:137], s[58:59] op_sel_hi:[1,1,0]\n"
        "v_pk_fma_f32 v[90:91], v[138:139], v[138:139], s[58:59] op_sel_hi:[1,1,0]\n"
        "v_pk_fma_f32 v[92:93], v[140:141], v[140:141], s[58:59] op_sel_hi:[1,1,0]\n"
        "v_pk_fma_f32 v[94:95], v[142:143], v[142:143], s[58:59] op_sel_hi:[1,1,0]\n"
        "v_pk_mul_f32 v[80:81], v[80:81], v[128:129] neg_lo:[0,1] neg_hi:[0,1]\n"
        "v_pk_mul_f32 v[82:83], v[82:83], v[130:131] neg_lo:[0,1] neg_hi:[0,1]\n"
        "v_pk_mul_f32 v[84:85], v[84:85], v[132:133] neg_lo:[0,1] neg_hi:[0,1]\n"
        "v_pk_mul_f32 v[86:87], v[86:87], v[134:135] neg_lo:[0,1] neg_hi:[0,1]\n"
        "v_pk_mul_f32 v[88:89], v[88:89], v[136:137] neg_lo:[0,1] neg_hi:[0,1]\n"
        "v_pk_mul_f32 v[90:91], v[90:91], v[138:139] neg_lo:[0,1] neg_hi:[0,1]\n"
        "v_pk_mul_f32 v[92:93], v[92:93], v[140:141] neg_lo:[0,1] neg_hi:[0,1]\n"
        "v_pk_mul_f32 v[94:95], v[94:95], v[142:143] neg_lo:[0,1] neg_hi:[0,1]\n"
        "s_waitcnt lgkmcnt(0)\n"
        "v_mfma_f32_32x32x16_bf16 v[144:159], v[184:187], v[16:19], 0\n"
        "v_cvt_pk_bf16_f32 v40, v96, v97\n"
        "v_cvt_pk_bf16_f32 v41, v98, v99\n"
        "v_exp_f32 v80, v80\n"
        "v_exp_f32_e64 v81, v81\n"
        "v_mfma_f32_32x32x16_bf16 v[144:159], v[188:191], v[20:23], v[144:159]\n"
        "v_cvt_pk_bf16_f32 v42, v100, v101\n"
        "v_cvt_pk_bf16_f32 v43, v102, v103\n"
        "v_exp_f32 v82, v82\n"
        "v_exp_f32 v83, v83\n"
        "v_mfma_f32_32x32x16_bf16 v[144:159], v[192:195], v[24:27], v[144:159]\n"
        "v_cvt_pk_bf16_f32 v44, v104, v105\n"
        "v_cvt_pk_bf16_f32 v45, v106, v107\n"
        "v_exp_f32 v84, v84\n"
        "v_exp_f32 v85, v85\n"
        "v_mfma_f32_32x32x16_bf16 v[144:159], v[196:199], v[28:31], v[144:159]\n"
        "v_cvt_pk_bf16_f32 v46, v108, v109\n"
        "v_cvt_pk_bf16_f32 v47, v110, v111\n"
        "v_exp_f32 v86, v86\n"
        "v_exp_f32 v87, v87\n"
        "v_mfma_f32_32x32x16_bf16 v[144:159], v[200:203], v[32:35], v[144:159]\n"
        "v_exp_f32 v88, v88\n"
        "v_exp_f32 v89, v89\n"
        "v_exp_f32_e64 v90, v90\n"
        "v_mfma_f32_32x32x16_bf16 v[144:159], v[204:207], v[36:39], v[144:159]\n"
        "v_exp_f32 v91, v91\n"
        "v_exp_f32 v92, v92\n"
        "v_exp_f32_e64 v93, v93\n"
        "v_mfma_f32_32x32x16_bf16 v[208:223], v[160:163], v[40:43], v[208:223]\n"
        "v_exp_f32 v94, v94\n"
        "v_exp_f32 v95, v95\n"
        "v_rcp_f32_e64 v64, v64\n"
        "v_mfma_f32_32x32x16_bf16 v[208:223], v[172:175], v[44:47], v[208:223]\n"
        "v_rcp_f32 v65, v65\n"
        "v_rcp_f32 v66, v66\n"
        "v_rcp_f32_e64 v67, v67\n"
        "v_mfma_f32_32x32x16_bf16 v[224:239], v[164:167], v[40:43], v[224:239]\n"
        "v_rcp_f32 v68, v68\n"
        "v_rcp_f32 v69, v69\n"
        "v_rcp_f32_e64 v70, v70\n"
        "v_mfma_f32_32x32x16_bf16 v[224:239], v[176:179], v[44:47], v[224:239]\n"
        "v_rcp_f32 v71, v71\n"
        "v_rcp_f32 v72, v72\n"
        "v_rcp_f32_e64 v73, v73\n"
        "v_mfma_f32_32x32x16_bf16 v[240:255], v[168:171], v[40:43], v[240:255]\n"
        "v_rcp_f32 v74, v74\n"
        "v_rcp_f32 v75, v75\n"
        "v_rcp_f32_e64 v76, v76\n"
        "v_mfma_f32_32x32x16_bf16 v[240:255], v[180:183], v[44:47], v[240:255]\n"
        "v_rcp_f32 v77, v77\n"
        "v_rcp_f32 v78, v78\n"
        "v_rcp_f32 v79, v79\n"
        "s_sub_u32 s52, s52, 1\n"
        "s_cbranch_scc0 .L19_%=\n"
        "v_add_u32 v6, s51, v6\n"
        "ds_read_b64_tr_b16 v[160:161], v6 offset:0\n"
        "ds_read_b64_tr_b16 v[162:163], v6 offset:1536\n"
        "ds_read_b64_tr_b16 v[164:165], v6 offset:64\n"
        "ds_read_b64_tr_b16 v[166:167], v6 offset:1600\n"
        "ds_read_b64_tr_b16 v[168:169], v6 offset:128\n"
        "ds_read_b64_tr_b16 v[170:171], v6 offset:1664\n"
        "ds_read_b64_tr_b16 v[172:173], v6 offset:3072\n"
        "ds_read_b64_tr_b16 v[174:175], v6 offset:4608\n"
        "ds_read_b64_tr_b16 v[176:177], v6 offset:3136\n"
        "ds_read_b64_tr_b16 v[178:179], v6 offset:4672\n"
        "ds_read_b64_tr_b16 v[180:181], v6 offset:3200\n"
        "ds_read_b64_tr_b16 v[182:183], v6 offset:4736\n"
        "ds_read_b64_tr_b16 v[184:185], v6 offset:6144\n"
        "ds_read_b64_tr_b16 v[186:187], v6 offset:7680\n"
        "ds_read_b64_tr_b16 v[188:189], v6 offset:6208\n"
        "ds_read_b64_tr_b16 v[190:191], v6 offset:7744\n"
        "ds_read_b64_tr_b16 v[192:193], v6 offset:6272\n"
        "ds_read_b64_tr_b16 v[194:195], v6 offset:7808\n"
        "ds_read_b64_tr_b16 v[196:197], v6 offset:9216\n"
        "ds_read_b64_tr_b16 v[198:199], v6 offset:10752\n"
        "ds_read_b64_tr_b16 v[200:201], v6 offset:9280\n"
        "ds_read_b64_tr_b16 v[202:203], v6 offset:10816\n"
        "ds_read_b64_tr_b16 v[204:205], v6 offset:9344\n"
        "ds_read_b64_tr_b16 v[206:207], v6 offset:10880\n"
        "v_pk_fma_f32 v[80:81], v[80:81], s[56:57], s[56:57] op_sel_hi:[1,0,0]\n"
        "v_pk_fma_f32 v[82:83], v[82:83], s[56:57], s[56:57] op_sel_hi:[1,0,0]\n"
        "v_pk_fma_f32 v[84:85], v[84:85], s[56:57], s[56:57] op_sel_hi:[1,0,0]\n"
        "v_pk_fma_f32 v[86:87], v[86:87], s[56:57], s[56:57] op_sel_hi:[1,0,0]\n"
        "v_pk_fma_f32 v[88:89], v[88:89], s[56:57], s[56:57] op_sel_hi:[1,0,0]\n"
        "v_pk_fma_f32 v[90:91], v[90:91], s[56:57], s[56:57] op_sel_hi:[1,0,0]\n"
        "v_pk_fma_f32 v[92:93], v[92:93], s[56:57], s[56:57] op_sel_hi:[1,0,0]\n"
        "v_pk_fma_f32 v[94:95], v[94:95], s[56:57], s[56:57] op_sel_hi:[1,0,0]\n"
        "v_pk_mul_f32 v[64:65], v[64:65], v[112:113]\n"
        "v_pk_mul_f32 v[66:67], v[66:67], v[114:115]\n"
        "v_pk_mul_f32 v[68:69], v[68:69], v[116:117]\n"
        "v_pk_mul_f32 v[70:71], v[70:71], v[118:119]\n"
        "v_pk_mul_f32 v[72:73], v[72:73], v[120:121]\n"
        "v_pk_mul_f32 v[74:75], v[74:75], v[122:123]\n"
        "v_pk_mul_f32 v[76:77], v[76:77], v[124:125]\n"
        "v_pk_mul_f32 v[78:79], v[78:79], v[126:127]\n"
        "v_pk_fma_f32 v[96:97], v[144:145], v[144:145], s[58:59] op_sel_hi:[1,1,0]\n"
        "v_pk_fma_f32 v[98:99], v[146:147], v[146:147], s[58:59] op_sel_hi:[1,1,0]\n"
        "v_pk_fma_f32 v[100:101], v[148:149], v[148:149], s[58:59] op_sel_hi:[1,1,0]\n"
        "v_pk_fma_f32 v[102:103], v[150:151], v[150:151], s[58:59] op_sel_hi:[1,1,0]\n"
        "v_pk_fma_f32 v[104:105], v[152:153], v[152:153], s[58:59] op_sel_hi:[1,1,0]\n"
        "v_pk_fma_f32 v[106:107], v[154:155], v[154:155], s[58:59] op_sel_hi:[1,1,0]\n"
        "v_pk_fma_f32 v[108:109], v[156:157], v[156:157], s[58:59] op_sel_hi:[1,1,0]\n"
        "v_pk_fma_f32 v[110:111], v[158:159], v[158:159], s[58:59] op_sel_hi:[1,1,0]\n"
        "v_pk_mul_f32 v[96:97], v[96:97], v[144:145] neg_lo:[0,1] neg_hi:[0,1]\n"
        "v_pk_mul_f32 v[98:99], v[98:99], v[146:147] neg_lo:[0,1] neg_hi:[0,1]\n"
        "v_pk_mul_f32 v[100:101], v[100:101], v[148:149] neg_lo:[0,1] neg_hi:[0,1]\n"
        "v_pk_mul_f32 v[102:103], v[102:103], v[150:151] neg_lo:[0,1] neg_hi:[0,1]\n"
        "v_pk_mul_f32 v[104:105], v[104:105], v[152:153] neg_lo:[0,1] neg_hi:[0,1]\n"
        "v_pk_mul_f32 v[106:107], v[106:107], v[154:155] neg_lo:[0,1] neg_hi:[0,1]\n"
        "v_pk_mul_f32 v[108:109], v[108:109], v[156:157] neg_lo:[0,1] neg_hi:[0,1]\n"
        "v_pk_mul_f32 v[110:111], v[110:111], v[158:159] neg_lo:[0,1] neg_hi:[0,1]\n"
        "s_waitcnt lgkmcnt(0)\n"
        "s_cmp_lt_u32 s20, 4\n"
        "s_cbranch_scc0 .L22_%=\n"
        "s_setprio 0\n"
        "v_cvt_pk_bf16_f32 v40, v64, v65\n"
        "v_cvt_pk_bf16_f32 v41, v66, v67\n"
        "v_cvt_pk_bf16_f32 v42, v68, v69\n"
        "v_cvt_pk_bf16_f32 v43, v70, v71\n"
        "v_cvt_pk_bf16_f32 v44, v72, v73\n"
        "v_cvt_pk_bf16_f32 v45, v74, v75\n"
        "v_mfma_f32_32x32x16_bf16 v[208:223], v[160:163], v[40:43], v[208:223]\n"
        "v_cvt_pk_bf16_f32 v46, v76, v77\n"
        "v_cvt_pk_bf16_f32 v47, v78, v79\n"
        "v_rcp_f32 v80, v80\n"
        "v_rcp_f32 v81, v81\n"
        "v_rcp_f32 v82, v82\n"
        "v_rcp_f32 v83, v83\n"
        "v_mfma_f32_32x32x16_bf16 v[224:239], v[164:167], v[40:43], v[224:239]\n"
        "v_rcp_f32 v84, v84\n"
        "v_rcp_f32 v85, v85\n"
        "v_rcp_f32 v86, v86\n"
        "v_rcp_f32 v87, v87\n"
        "v_rcp_f32 v88, v88\n"
        "v_rcp_f32 v89, v89\n"
        "v_mfma_f32_32x32x16_bf16 v[240:255], v[168:171], v[40:43], v[240:255]\n"
        "v_rcp_f32 v90, v90\n"
        "v_rcp_f32 v91, v91\n"
        "v_rcp_f32 v92, v92\n"
        "v_rcp_f32 v93, v93\n"
        "v_rcp_f32_e64 v94, v94\n"
        "v_mfma_f32_32x32x16_bf16 v[208:223], v[172:175], v[44:47], v[208:223]\n"
        "v_rcp_f32 v95, v95\n"
        "v_exp_f32 v96, v96\n"
        "v_exp_f32 v97, v97\n"
        "v_exp_f32 v98, v98\n"
        "v_exp_f32 v99, v99\n"
        "v_exp_f32 v100, v100\n"
        "v_mfma_f32_32x32x16_bf16 v[224:239], v[176:179], v[44:47], v[224:239]\n"
        "v_exp_f32 v101, v101\n"
        "v_exp_f32 v102, v102\n"
        "v_exp_f32 v103, v103\n"
        "v_exp_f32 v104, v104\n"
        "v_exp_f32_e64 v105, v105\n"
        "v_mfma_f32_32x32x16_bf16 v[240:255], v[180:183], v[44:47], v[240:255]\n"
        "v_exp_f32 v106, v106\n"
        "v_exp_f32 v107, v107\n"
        "v_exp_f32 v108, v108\n"
        "v_exp_f32 v109, v109\n"
        "v_exp_f32 v110, v110\n"
        "v_exp_f32 v111, v111\n"
        "ds_read_b64_tr_b16 v[160:161], v6 offset:12288\n"
        "ds_read_b64_tr_b16 v[162:163], v6 offset:13824\n"
        "ds_read_b64_tr_b16 v[164:165], v6 offset:12352\n"
        "ds_read_b64_tr_b16 v[166:167], v6 offset:13888\n"
        "ds_read_b64_tr_b16 v[168:169], v6 offset:12416\n"
        "ds_read_b64_tr_b16 v[170:171], v6 offset:13952\n"
        "ds_read_b64_tr_b16 v[172:173], v6 offset:15360\n"
        "ds_read_b64_tr_b16 v[174:175], v6 offset:16896\n"
        "ds_read_b64_tr_b16 v[176:177], v6 offset:15424\n"
        "ds_read_b64_tr_b16 v[178:179], v6 offset:16960\n"
        "ds_read_b64_tr_b16 v[180:181], v6 offset:15488\n"
        "ds_read_b64_tr_b16 v[182:183], v6 offset:17024\n"
        "v_pk_mul_f32 v[80:81], v[80:81], v[128:129]\n"
        "v_pk_mul_f32 v[82:83], v[82:83], v[130:131]\n"
        "v_pk_mul_f32 v[84:85], v[84:85], v[132:133]\n"
        "v_pk_mul_f32 v[86:87], v[86:87], v[134:135]\n"
        "v_pk_mul_f32 v[88:89], v[88:89], v[136:137]\n"
        "v_pk_mul_f32 v[90:91], v[90:91], v[138:139]\n"
        "v_pk_mul_f32 v[92:93], v[92:93], v[140:141]\n"
        "v_pk_mul_f32 v[94:95], v[94:95], v[142:143]\n"
        "v_pk_fma_f32 v[96:97], v[96:97], s[56:57], s[56:57] op_sel_hi:[1,0,0]\n"
        "v_pk_fma_f32 v[98:99], v[98:99], s[56:57], s[56:57] op_sel_hi:[1,0,0]\n"
        "v_pk_fma_f32 v[100:101], v[100:101], s[56:57], s[56:57] op_sel_hi:[1,0,0]\n"
        "v_pk_fma_f32 v[102:103], v[102:103], s[56:57], s[56:57] op_sel_hi:[1,0,0]\n"
        "v_pk_fma_f32 v[104:105], v[104:105], s[56:57], s[56:57] op_sel_hi:[1,0,0]\n"
        "v_pk_fma_f32 v[106:107], v[106:107], s[56:57], s[56:57] op_sel_hi:[1,0,0]\n"
        "v_pk_fma_f32 v[108:109], v[108:109], s[56:57], s[56:57] op_sel_hi:[1,0,0]\n"
        "v_pk_fma_f32 v[110:111], v[110:111], s[56:57], s[56:57] op_sel_hi:[1,0,0]\n"
        "v_cvt_pk_bf16_f32 v40, v80, v81\n"
        "v_cvt_pk_bf16_f32 v41, v82, v83\n"
        "v_cvt_pk_bf16_f32 v42, v84, v85\n"
        "v_cvt_pk_bf16_f32 v43, v86, v87\n"
        "v_cvt_pk_bf16_f32 v44, v88, v89\n"
        "v_cvt_pk_bf16_f32 v45, v90, v91\n"
        "v_mfma_f32_32x32x16_bf16 v[208:223], v[184:187], v[40:43], v[208:223]\n"
        "v_cvt_pk_bf16_f32 v46, v92, v93\n"
        "v_cvt_pk_bf16_f32 v47, v94, v95\n"
        "v_rcp_f32 v96, v96\n"
        "v_rcp_f32 v97, v97\n"
        "v_mfma_f32_32x32x16_bf16 v[224:239], v[188:191], v[40:43], v[224:239]\n"
        "v_rcp_f32 v98, v98\n"
        "v_rcp_f32 v99, v99\n"
        "v_rcp_f32_e64 v100, v100\n"
        "v_mfma_f32_32x32x16_bf16 v[240:255], v[192:195], v[40:43], v[240:255]\n"
        "v_rcp_f32 v101, v101\n"
        "v_rcp_f32 v102, v102\n"
        "v_rcp_f32_e64 v103, v103\n"
        "v_mfma_f32_32x32x16_bf16 v[208:223], v[196:199], v[44:47], v[208:223]\n"
        "v_rcp_f32 v104, v104\n"
        "v_rcp_f32 v105, v105\n"
        "v_rcp_f32_e64 v106, v106\n"
        "v_mfma_f32_32x32x16_bf16 v[224:239], v[200:203], v[44:47], v[224:239]\n"
        "v_rcp_f32 v107, v107\n"
        "v_rcp_f32 v108, v108\n"
        "v_rcp_f32_e64 v109, v109\n"
        "v_mfma_f32_32x32x16_bf16 v[240:255], v[204:207], v[44:47], v[240:255]\n"
        "v_rcp_f32 v110, v110\n"
        "v_rcp_f32 v111, v111\n"
        "v_pk_mul_f32 v[96:97], v[96:97], v[144:145]\n"
        "v_pk_mul_f32 v[98:99], v[98:99], v[146:147]\n"
        "v_pk_mul_f32 v[100:101], v[100:101], v[148:149]\n"
        "v_pk_mul_f32 v[102:103], v[102:103], v[150:151]\n"
        "v_pk_mul_f32 v[104:105], v[104:105], v[152:153]\n"
        "v_pk_mul_f32 v[106:107], v[106:107], v[154:155]\n"
        "v_pk_mul_f32 v[108:109], v[108:109], v[156:157]\n"
        "v_pk_mul_f32 v[110:111], v[110:111], v[158:159]\n"
        "s_waitcnt lgkmcnt(0)\n"
        "v_cvt_pk_bf16_f32 v40, v96, v97\n"
        "v_cvt_pk_bf16_f32 v41, v98, v99\n"
        "v_cvt_pk_bf16_f32 v42, v100, v101\n"
        "v_cvt_pk_bf16_f32 v43, v102, v103\n"
        "v_cvt_pk_bf16_f32 v44, v104, v105\n"
        "v_cvt_pk_bf16_f32 v45, v106, v107\n"
        "v_cvt_pk_bf16_f32 v46, v108, v109\n"
        "v_cvt_pk_bf16_f32 v47, v110, v111\n"
        "s_nop 0\n"
        "v_mfma_f32_32x32x16_bf16 v[208:223], v[160:163], v[40:43], v[208:223]\n"
        "v_mfma_f32_32x32x16_bf16 v[208:223], v[172:175], v[44:47], v[208:223]\n"
        "v_mfma_f32_32x32x16_bf16 v[224:239], v[164:167], v[40:43], v[224:239]\n"
        "v_mfma_f32_32x32x16_bf16 v[224:239], v[176:179], v[44:47], v[224:239]\n"
        "s_nop 5\n"
        "v_mfma_f32_32x32x16_bf16 v[240:255], v[168:171], v[40:43], v[240:255]\n"
        "s_nop 2\n"
        "v_cvt_pk_bf16_f32 v128, v208, v209\n"
        "v_cvt_pk_bf16_f32 v129, v210, v211\n"
        "v_mfma_f32_32x32x16_bf16 v[240:255], v[180:183], v[44:47], v[240:255]\n"
        "v_cvt_pk_bf16_f32 v130, v212, v213\n"
        "v_cvt_pk_bf16_f32 v131, v214, v215\n"
        "v_cvt_pk_bf16_f32 v132, v216, v217\n"
        "v_cvt_pk_bf16_f32 v133, v218, v219\n"
        "v_cvt_pk_bf16_f32 v134, v220, v221\n"
        "v_cvt_pk_bf16_f32 v135, v222, v223\n"
        "s_nop 0\n"
        "v_permlane16_swap_b32 v128, v132\n"
        "v_permlane16_swap_b32 v129, v133\n"
        "v_permlane16_swap_b32 v130, v134\n"
        "v_permlane16_swap_b32_e64 v131, v135\n"
        "buffer_store_dwordx4 v[128:131], v1, s[40:43], 0 offen offset:0 nt\n"
        "buffer_store_dwordx4 v[132:135], v15, s[40:43], 0 offen offset:0 nt\n"
        "v_cvt_pk_bf16_f32 v136, v224, v225\n"
        "v_cvt_pk_bf16_f32 v137, v226, v227\n"
        "v_cvt_pk_bf16_f32 v138, v228, v229\n"
        "v_cvt_pk_bf16_f32 v139, v230, v231\n"
        "v_cvt_pk_bf16_f32 v140, v232, v233\n"
        "v_cvt_pk_bf16_f32 v141, v234, v235\n"
        "v_cvt_pk_bf16_f32 v142, v236, v237\n"
        "v_cvt_pk_bf16_f32 v143, v238, v239\n"
        "s_nop 0\n"
        "v_permlane16_swap_b32 v136, v140\n"
        "v_permlane16_swap_b32 v137, v141\n"
        "v_permlane16_swap_b32 v138, v142\n"
        "v_permlane16_swap_b32_e64 v139, v143\n"
        "buffer_store_dwordx4 v[136:139], v1, s[40:43], 0 offen offset:64 nt\n"
        "buffer_store_dwordx4 v[140:143], v15, s[40:43], 0 offen offset:64 nt\n"
        "v_cvt_pk_bf16_f32 v144, v240, v241\n"
        "v_cvt_pk_bf16_f32 v145, v242, v243\n"
        "v_cvt_pk_bf16_f32 v146, v244, v245\n"
        "v_cvt_pk_bf16_f32 v147, v246, v247\n"
        "v_cvt_pk_bf16_f32 v148, v248, v249\n"
        "v_cvt_pk_bf16_f32 v149, v250, v251\n"
        "v_cvt_pk_bf16_f32 v150, v252, v253\n"
        "v_cvt_pk_bf16_f32 v151, v254, v255\n"
        "s_nop 0\n"
        "v_permlane16_swap_b32 v144, v148\n"
        "v_permlane16_swap_b32 v145, v149\n"
        "v_permlane16_swap_b32 v146, v150\n"
        "v_permlane16_swap_b32_e64 v147, v151\n"
        "buffer_store_dwordx4 v[144:147], v1, s[40:43], 0 offen offset:128 nt\n"
        "buffer_store_dwordx4 v[148:151], v15, s[40:43], 0 offen offset:128 nt\n"
        "s_branch .L13_%=\n"
        ".L22_%=:\n"
        "s_cmp_ge_u32 s20, 4\n"
        "s_cbranch_scc0 .L23_%=\n"
        "s_setprio 1\n"
        "v_mov_b64 v[112:113], 0\n"
        "v_mov_b64 v[114:115], 0\n"
        "v_mov_b64 v[116:117], 0\n"
        "v_mov_b64 v[118:119], 0\n"
        "v_mov_b64 v[120:121], 0\n"
        "v_mov_b64 v[122:123], 0\n"
        "v_mov_b64 v[124:125], 0\n"
        "v_mov_b64 v[126:127], 0\n"
        "s_lshl_b32 s45, s54, 2\n"
        "v_add_u32 v13, s45, v52\n"
        "s_cmp_eq_u32 s55, 1\n"
        "s_cbranch_scc0 .L24_%=\n"
        "s_mov_b32 exec_lo, 0xf0000f\n"
        "s_mov_b32 exec_hi, 0xf0000f\n"
        "ds_read_b128 v[112:115], v10 offset:0\n"
        "ds_read_b128 v[116:119], v10 offset:128\n"
        "ds_read_b128 v[120:123], v10 offset:256\n"
        "ds_read_b128 v[124:127], v10 offset:384\n"
        "s_nop 0\n"
        "s_mov_b64 exec, -1\n"
        "v_cvt_pk_bf16_f32 v40, v64, v65\n"
        "v_cvt_pk_bf16_f32 v41, v66, v67\n"
        "v_cvt_pk_bf16_f32 v42, v68, v69\n"
        "v_cvt_pk_bf16_f32 v43, v70, v71\n"
        "v_cvt_pk_bf16_f32 v44, v72, v73\n"
        "v_cvt_pk_bf16_f32 v45, v74, v75\n"
        "v_mfma_f32_32x32x16_bf16 v[208:223], v[160:163], v[40:43], v[208:223]\n"
        "v_cvt_pk_bf16_f32 v46, v76, v77\n"
        "v_cvt_pk_bf16_f32 v47, v78, v79\n"
        "v_rcp_f32 v80, v80\n"
        "v_rcp_f32 v81, v81\n"
        "v_rcp_f32_e64 v82, v82\n"
        "v_mfma_f32_32x32x16_bf16 v[224:239], v[164:167], v[40:43], v[224:239]\n"
        "v_rcp_f32 v83, v83\n"
        "v_rcp_f32 v84, v84\n"
        "v_rcp_f32_e64 v85, v85\n"
        "v_mfma_f32_32x32x16_bf16 v[240:255], v[168:171], v[40:43], v[240:255]\n"
        "v_rcp_f32 v86, v86\n"
        "v_rcp_f32 v87, v87\n"
        "v_rcp_f32 v88, v88\n"
        "v_rcp_f32 v89, v89\n"
        "v_mfma_f32_32x32x16_bf16 v[208:223], v[172:175], v[44:47], v[208:223]\n"
        "v_rcp_f32 v90, v90\n"
        "v_rcp_f32 v91, v91\n"
        "v_rcp_f32_e64 v92, v92\n"
        "v_rcp_f32 v93, v93\n"
        "s_waitcnt lgkmcnt(0)\n"
        "v_mfma_f32_16x16x32_bf16 v[64:67], v[160:163], v[112:115], 0\n"
        "v_rcp_f32_e64 v94, v94\n"
        "v_mfma_f32_32x32x16_bf16 v[224:239], v[176:179], v[44:47], v[224:239]\n"
        "v_rcp_f32 v95, v95\n"
        "v_exp_f32 v96, v96\n"
        "v_exp_f32 v97, v97\n"
        "v_exp_f32 v98, v98\n"
        "v_mfma_f32_16x16x32_bf16 v[68:71], v[164:167], v[112:115], 0\n"
        "v_exp_f32 v99, v99\n"
        "v_exp_f32 v100, v100\n"
        "v_mfma_f32_32x32x16_bf16 v[240:255], v[180:183], v[44:47], v[240:255]\n"
        "v_exp_f32 v101, v101\n"
        "v_exp_f32 v102, v102\n"
        "v_exp_f32 v103, v103\n"
        "v_exp_f32 v104, v104\n"
        "v_mfma_f32_16x16x32_bf16 v[72:75], v[168:171], v[112:115], 0\n"
        "v_exp_f32_e64 v105, v105\n"
        "v_mfma_f32_16x16x32_bf16 v[64:67], v[172:175], v[116:119], v[64:67]\n"
        "v_exp_f32 v106, v106\n"
        "v_exp_f32 v107, v107\n"
        "v_mfma_f32_16x16x32_bf16 v[68:71], v[176:179], v[116:119], v[68:71]\n"
        "v_exp_f32 v108, v108\n"
        "v_exp_f32 v109, v109\n"
        "v_mfma_f32_16x16x32_bf16 v[72:75], v[180:183], v[116:119], v[72:75]\n"
        "v_exp_f32 v110, v110\n"
        "v_exp_f32 v111, v111\n"
        "ds_read_b64_tr_b16 v[160:161], v6 offset:12288\n"
        "ds_read_b64_tr_b16 v[162:163], v6 offset:13824\n"
        "ds_read_b64_tr_b16 v[164:165], v6 offset:12352\n"
        "ds_read_b64_tr_b16 v[166:167], v6 offset:13888\n"
        "ds_read_b64_tr_b16 v[168:169], v6 offset:12416\n"
        "ds_read_b64_tr_b16 v[170:171], v6 offset:13952\n"
        "ds_read_b64_tr_b16 v[172:173], v6 offset:15360\n"
        "ds_read_b64_tr_b16 v[174:175], v6 offset:16896\n"
        "ds_read_b64_tr_b16 v[176:177], v6 offset:15424\n"
        "ds_read_b64_tr_b16 v[178:179], v6 offset:16960\n"
        "ds_read_b64_tr_b16 v[180:181], v6 offset:15488\n"
        "ds_read_b64_tr_b16 v[182:183], v6 offset:17024\n"
        "s_mov_b32 exec_lo, 0xf0000f\n"
        "s_mov_b32 exec_hi, 0xf0000f\n"
        "ds_read_b128 v[112:115], v10 offset:512\n"
        "ds_read_b128 v[116:119], v10 offset:640\n"
        "s_nop 0\n"
        "s_mov_b64 exec, -1\n"
        "v_pk_mul_f32 v[80:81], v[80:81], v[128:129]\n"
        "v_pk_mul_f32 v[82:83], v[82:83], v[130:131]\n"
        "v_pk_mul_f32 v[84:85], v[84:85], v[132:133]\n"
        "v_pk_mul_f32 v[86:87], v[86:87], v[134:135]\n"
        "v_pk_mul_f32 v[88:89], v[88:89], v[136:137]\n"
        "v_pk_mul_f32 v[90:91], v[90:91], v[138:139]\n"
        "v_pk_mul_f32 v[92:93], v[92:93], v[140:141]\n"
        "v_pk_mul_f32 v[94:95], v[94:95], v[142:143]\n"
        "v_pk_fma_f32 v[96:97], v[96:97], s[56:57], s[56:57] op_sel_hi:[1,0,0]\n"
        "v_pk_fma_f32 v[98:99], v[98:99], s[56:57], s[56:57] op_sel_hi:[1,0,0]\n"
        "v_pk_fma_f32 v[100:101], v[100:101], s[56:57], s[56:57] op_sel_hi:[1,0,0]\n"
        "v_pk_fma_f32 v[102:103], v[102:103], s[56:57], s[56:57] op_sel_hi:[1,0,0]\n"
        "v_pk_fma_f32 v[104:105], v[104:105], s[56:57], s[56:57] op_sel_hi:[1,0,0]\n"
        "v_pk_fma_f32 v[106:107], v[106:107], s[56:57], s[56:57] op_sel_hi:[1,0,0]\n"
        "v_pk_fma_f32 v[108:109], v[108:109], s[56:57], s[56:57] op_sel_hi:[1,0,0]\n"
        "v_pk_fma_f32 v[110:111], v[110:111], s[56:57], s[56:57] op_sel_hi:[1,0,0]\n"
        "v_mfma_f32_16x16x32_bf16 v[64:67], v[184:187], v[120:123], v[64:67]\n"
        "v_cvt_pk_bf16_f32 v40, v80, v81\n"
        "v_cvt_pk_bf16_f32 v41, v82, v83\n"
        "v_and_b32_e64 v55, 3, v0\n"
        "v_mfma_f32_16x16x32_bf16 v[68:71], v[188:191], v[120:123], v[68:71]\n"
        "v_cvt_pk_bf16_f32 v42, v84, v85\n"
        "v_cvt_pk_bf16_f32 v43, v86, v87\n"
        "v_rcp_f32_e64 v96, v96\n"
        "v_mul_u32_u24 v12, 192, v55\n"
        "v_mfma_f32_16x16x32_bf16 v[72:75], v[192:195], v[120:123], v[72:75]\n"
        "v_cvt_pk_bf16_f32 v44, v88, v89\n"
        "v_cvt_pk_bf16_f32 v45, v90, v91\n"
        "v_and_b32_e64 v55, 16, v0\n"
        "v_mfma_f32_16x16x32_bf16 v[64:67], v[196:199], v[124:127], v[64:67]\n"
        "v_cvt_pk_bf16_f32 v46, v92, v93\n"
        "v_cvt_pk_bf16_f32 v47, v94, v95\n"
        "v_rcp_f32_e64 v97, v97\n"
        "v_lshl_add_u32 v12, v55, 1, v12\n"
        "v_mfma_f32_16x16x32_bf16 v[68:71], v[200:203], v[124:127], v[68:71]\n"
        "v_rcp_f32 v98, v98\n"
        "v_and_b32 v55, 4, v0\n"
        "v_mfma_f32_16x16x32_bf16 v[72:75], v[204:207], v[124:127], v[72:75]\n"
        "v_rcp_f32 v99, v99\n"
        "v_rcp_f32 v100, v100\n"
        "v_lshl_add_u32 v12, v55, 2, v12\n"
        "s_waitcnt lgkmcnt(0)\n"
        "v_mfma_f32_16x16x32_bf16 v[64:67], v[160:163], v[112:115], v[64:67]\n"
        "v_rcp_f32 v101, v101\n"
        "v_bfe_u32 v55, v0, 5, 1\n"
        "v_mfma_f32_16x16x32_bf16 v[68:71], v[164:167], v[112:115], v[68:71]\n"
        "v_rcp_f32 v102, v102\n"
        "v_rcp_f32 v103, v103\n"
        "v_lshl_add_u32 v12, v55, 3, v12\n"
        "v_mfma_f32_16x16x32_bf16 v[72:75], v[168:171], v[112:115], v[72:75]\n"
        "v_rcp_f32 v104, v104\n"
        "s_sub_u32 s46, s20, 4\n"
        "v_mfma_f32_16x16x32_bf16 v[64:67], v[172:175], v[116:119], v[64:67]\n"
        "v_rcp_f32 v105, v105\n"
        "v_rcp_f32 v106, v106\n"
        "s_mul_i32 s46, s46, 768\n"
        "v_mfma_f32_16x16x32_bf16 v[68:71], v[176:179], v[116:119], v[68:71]\n"
        "v_rcp_f32_e64 v107, v107\n"
        "s_add_u32 s46, s46, 159744\n"
        "v_mfma_f32_16x16x32_bf16 v[72:75], v[180:183], v[116:119], v[72:75]\n"
        "v_rcp_f32 v108, v108\n"
        "v_rcp_f32 v109, v109\n"
        "v_add_u32_e64 v12, s46, v12\n"
        "v_mfma_f32_32x32x16_bf16 v[208:223], v[184:187], v[40:43], v[208:223]\n"
        "v_rcp_f32 v110, v110\n"
        "v_rcp_f32 v111, v111\n"
        "v_mfma_f32_32x32x16_bf16 v[224:239], v[188:191], v[40:43], v[224:239]\n"
        "v_mfma_f32_32x32x16_bf16 v[240:255], v[192:195], v[40:43], v[240:255]\n"
        "v_mfma_f32_32x32x16_bf16 v[208:223], v[196:199], v[44:47], v[208:223]\n"
        "v_mfma_f32_32x32x16_bf16 v[224:239], v[200:203], v[44:47], v[224:239]\n"
        "v_mfma_f32_32x32x16_bf16 v[240:255], v[204:207], v[44:47], v[240:255]\n"
        "v_cvt_pk_bf16_f32 v80, v64, v65\n"
        "v_cvt_pk_bf16_f32 v81, v66, v67\n"
        "v_cvt_pk_bf16_f32 v82, v68, v69\n"
        "v_cvt_pk_bf16_f32 v83, v70, v71\n"
        "v_cvt_pk_bf16_f32 v84, v72, v73\n"
        "v_cvt_pk_bf16_f32 v85, v74, v75\n"
        "s_mov_b32 exec_lo, 0xff00ff\n"
        "s_mov_b32 exec_hi, 0xff00ff\n"
        "ds_write_b64 v12, v[80:81] offset:0\n"
        "ds_write_b64 v12, v[82:83] offset:64\n"
        "ds_write_b64 v12, v[84:85] offset:128\n"
        "s_mov_b64 exec, 1\n"
        "ds_add_u32 v52, v53 offset:12\n"
        "s_mov_b64 exec, -1\n"
        "v_pk_mul_f32 v[96:97], v[96:97], v[144:145]\n"
        "v_pk_mul_f32 v[98:99], v[98:99], v[146:147]\n"
        "v_pk_mul_f32 v[100:101], v[100:101], v[148:149]\n"
        "v_pk_mul_f32 v[102:103], v[102:103], v[150:151]\n"
        "v_pk_mul_f32 v[104:105], v[104:105], v[152:153]\n"
        "v_pk_mul_f32 v[106:107], v[106:107], v[154:155]\n"
        "v_pk_mul_f32 v[108:109], v[108:109], v[156:157]\n"
        "v_pk_mul_f32 v[110:111], v[110:111], v[158:159]\n"
        "v_cvt_pk_bf16_f32 v40, v96, v97\n"
        "v_cvt_pk_bf16_f32 v41, v98, v99\n"
        "v_cvt_pk_bf16_f32 v42, v100, v101\n"
        "v_cvt_pk_bf16_f32 v43, v102, v103\n"
        "v_cvt_pk_bf16_f32 v44, v104, v105\n"
        "v_cvt_pk_bf16_f32 v45, v106, v107\n"
        "v_cvt_pk_bf16_f32 v46, v108, v109\n"
        "v_cvt_pk_bf16_f32 v47, v110, v111\n"
        "s_nop 0\n"
        "s_nop 0\n"
        "v_mfma_f32_32x32x16_bf16 v[240:255], v[168:171], v[40:43], v[240:255]\n"
        "v_mfma_f32_32x32x16_bf16 v[240:255], v[180:183], v[44:47], v[240:255]\n"
        "v_mfma_f32_32x32x16_bf16 v[208:223], v[160:163], v[40:43], v[208:223]\n"
        "v_mfma_f32_32x32x16_bf16 v[208:223], v[172:175], v[44:47], v[208:223]\n"
        "s_nop 5\n"
        "v_mfma_f32_32x32x16_bf16 v[224:239], v[164:167], v[40:43], v[224:239]\n"
        "s_nop 2\n"
        "v_cvt_pk_bf16_f32 v144, v240, v241\n"
        "v_cvt_pk_bf16_f32 v145, v242, v243\n"
        "v_mfma_f32_32x32x16_bf16 v[224:239], v[176:179], v[44:47], v[224:239]\n"
        "v_cvt_pk_bf16_f32 v146, v244, v245\n"
        "v_cvt_pk_bf16_f32 v147, v246, v247\n"
        "v_cvt_pk_bf16_f32 v148, v248, v249\n"
        "v_cvt_pk_bf16_f32 v149, v250, v251\n"
        "v_cvt_pk_bf16_f32 v150, v252, v253\n"
        "v_cvt_pk_bf16_f32 v151, v254, v255\n"
        "ds_write_b128 v11, v[144:147] offset:4096\n"
        "ds_write_b128 v11, v[148:151] offset:5120\n"
        "s_mov_b64 exec, 1\n"
        "ds_add_u32 v13, v53 offset:8\n"
        "s_mov_b64 exec, -1\n"
        "ds_read_b32 v54, v13 offset:0\n"
        "s_waitcnt lgkmcnt(0)\n"
        "v_readfirstlane_b32 s23, v54\n"
        "s_cmp_ge_u32 s23, 1\n"
        "s_cbranch_scc0 .L25_%=\n"
        ".L26_%=:\n"
        "ds_read_b128 v[16:19], v11 offset:0\n"
        "ds_read_b128 v[20:23], v11 offset:1024\n"
        "ds_read_b128 v[24:27], v11 offset:2048\n"
        "ds_read_b128 v[28:31], v11 offset:3072\n"
        "s_waitcnt lgkmcnt(2)\n"
        "v_mfma_f32_32x32x16_bf16 v[208:223], v[56:59], v[16:19], v[208:223]\n"
        "v_mfma_f32_32x32x16_bf16 v[208:223], v[60:63], v[20:23], v[208:223]\n"
        "s_waitcnt lgkmcnt(0)\n"
        "v_mfma_f32_32x32x16_bf16 v[224:239], v[56:59], v[24:27], v[224:239]\n"
        "v_mfma_f32_32x32x16_bf16 v[224:239], v[60:63], v[28:31], v[224:239]\n"
        "s_nop 0\n"
        "s_nop 8\n"
        "v_cvt_pk_bf16_f32 v128, v208, v209\n"
        "v_cvt_pk_bf16_f32 v129, v210, v211\n"
        "v_cvt_pk_bf16_f32 v130, v212, v213\n"
        "v_cvt_pk_bf16_f32 v131, v214, v215\n"
        "v_cvt_pk_bf16_f32 v132, v216, v217\n"
        "v_cvt_pk_bf16_f32 v133, v218, v219\n"
        "v_cvt_pk_bf16_f32 v134, v220, v221\n"
        "v_cvt_pk_bf16_f32 v135, v222, v223\n"
        "s_nop 0\n"
        "v_permlane16_swap_b32 v128, v132\n"
        "v_permlane16_swap_b32 v129, v133\n"
        "v_permlane16_swap_b32 v130, v134\n"
        "v_permlane16_swap_b32_e64 v131, v135\n"
        "buffer_store_dwordx4 v[128:131], v1, s[40:43], 0 offen offset:0 nt\n"
        "buffer_store_dwordx4 v[132:135], v15, s[40:43], 0 offen offset:0 nt\n"
        "v_cvt_pk_bf16_f32 v136, v224, v225\n"
        "v_cvt_pk_bf16_f32 v137, v226, v227\n"
        "v_cvt_pk_bf16_f32 v138, v228, v229\n"
        "v_cvt_pk_bf16_f32 v139, v230, v231\n"
        "v_cvt_pk_bf16_f32 v140, v232, v233\n"
        "v_cvt_pk_bf16_f32 v141, v234, v235\n"
        "v_cvt_pk_bf16_f32 v142, v236, v237\n"
        "v_cvt_pk_bf16_f32 v143, v238, v239\n"
        "s_nop 0\n"
        "v_permlane16_swap_b32 v136, v140\n"
        "v_permlane16_swap_b32 v137, v141\n"
        "v_permlane16_swap_b32 v138, v142\n"
        "v_permlane16_swap_b32_e64 v139, v143\n"
        "buffer_store_dwordx4 v[136:139], v1, s[40:43], 0 offen offset:64 nt\n"
        "buffer_store_dwordx4 v[140:143], v15, s[40:43], 0 offen offset:64 nt\n"
        ".L24_%=:\n"
        "s_cmp_eq_u32 s55, 2\n"
        "s_cbranch_scc0 .L27_%=\n"
        "s_add_u32 s23, s44, 2922\n"
        "s_mul_i32 s44, s23, 192\n"
        "buffer_load_dwordx4 v[48:51], v2, s[36:39], s44 offen\n"
        "s_mov_b32 exec_lo, 0xf0000f\n"
        "s_mov_b32 exec_hi, 0xf0000f\n"
        "ds_read_b128 v[112:115], v10 offset:0\n"
        "ds_read_b128 v[116:119], v10 offset:128\n"
        "ds_read_b128 v[120:123], v10 offset:256\n"
        "ds_read_b128 v[124:127], v10 offset:384\n"
        "s_nop 0\n"
        "s_mov_b64 exec, -1\n"
        "v_cvt_pk_bf16_f32 v40, v64, v65\n"
        "v_cvt_pk_bf16_f32 v41, v66, v67\n"
        "v_cvt_pk_bf16_f32 v42, v68, v69\n"
        "v_cvt_pk_bf16_f32 v43, v70, v71\n"
        "v_cvt_pk_bf16_f32 v44, v72, v73\n"
        "v_cvt_pk_bf16_f32 v45, v74, v75\n"
        "v_mfma_f32_32x32x16_bf16 v[208:223], v[160:163], v[40:43], v[208:223]\n"
        "v_cvt_pk_bf16_f32 v46, v76, v77\n"
        "v_cvt_pk_bf16_f32 v47, v78, v79\n"
        "v_rcp_f32 v80, v80\n"
        "v_rcp_f32 v81, v81\n"
        "v_rcp_f32_e64 v82, v82\n"
        "v_mfma_f32_32x32x16_bf16 v[224:239], v[164:167], v[40:43], v[224:239]\n"
        "v_rcp_f32 v83, v83\n"
        "v_rcp_f32 v84, v84\n"
        "v_rcp_f32_e64 v85, v85\n"
        "v_mfma_f32_32x32x16_bf16 v[240:255], v[168:171], v[40:43], v[240:255]\n"
        "v_rcp_f32 v86, v86\n"
        "v_rcp_f32 v87, v87\n"
        "v_rcp_f32 v88, v88\n"
        "v_rcp_f32 v89, v89\n"
        "v_mfma_f32_32x32x16_bf16 v[208:223], v[172:175], v[44:47], v[208:223]\n"
        "v_rcp_f32 v90, v90\n"
        "v_rcp_f32 v91, v91\n"
        "v_rcp_f32_e64 v92, v92\n"
        "v_rcp_f32 v93, v93\n"
        "s_waitcnt lgkmcnt(0)\n"
        "v_mfma_f32_16x16x32_bf16 v[64:67], v[160:163], v[112:115], 0\n"
        "v_rcp_f32_e64 v94, v94\n"
        "v_mfma_f32_32x32x16_bf16 v[224:239], v[176:179], v[44:47], v[224:239]\n"
        "v_rcp_f32 v95, v95\n"
        "v_exp_f32 v96, v96\n"
        "v_exp_f32 v97, v97\n"
        "v_exp_f32 v98, v98\n"
        "v_mfma_f32_16x16x32_bf16 v[68:71], v[164:167], v[112:115], 0\n"
        "v_exp_f32 v99, v99\n"
        "v_exp_f32 v100, v100\n"
        "v_mfma_f32_32x32x16_bf16 v[240:255], v[180:183], v[44:47], v[240:255]\n"
        "v_exp_f32 v101, v101\n"
        "v_exp_f32 v102, v102\n"
        "v_exp_f32 v103, v103\n"
        "v_exp_f32 v104, v104\n"
        "v_mfma_f32_16x16x32_bf16 v[72:75], v[168:171], v[112:115], 0\n"
        "v_exp_f32_e64 v105, v105\n"
        "v_mfma_f32_16x16x32_bf16 v[64:67], v[172:175], v[116:119], v[64:67]\n"
        "v_exp_f32 v106, v106\n"
        "v_exp_f32 v107, v107\n"
        "v_mfma_f32_16x16x32_bf16 v[68:71], v[176:179], v[116:119], v[68:71]\n"
        "v_exp_f32 v108, v108\n"
        "v_exp_f32 v109, v109\n"
        "v_mfma_f32_16x16x32_bf16 v[72:75], v[180:183], v[116:119], v[72:75]\n"
        "v_exp_f32 v110, v110\n"
        "v_exp_f32 v111, v111\n"
        "ds_read_b64_tr_b16 v[160:161], v6 offset:12288\n"
        "ds_read_b64_tr_b16 v[162:163], v6 offset:13824\n"
        "ds_read_b64_tr_b16 v[164:165], v6 offset:12352\n"
        "ds_read_b64_tr_b16 v[166:167], v6 offset:13888\n"
        "ds_read_b64_tr_b16 v[168:169], v6 offset:12416\n"
        "ds_read_b64_tr_b16 v[170:171], v6 offset:13952\n"
        "ds_read_b64_tr_b16 v[172:173], v6 offset:15360\n"
        "ds_read_b64_tr_b16 v[174:175], v6 offset:16896\n"
        "ds_read_b64_tr_b16 v[176:177], v6 offset:15424\n"
        "ds_read_b64_tr_b16 v[178:179], v6 offset:16960\n"
        "ds_read_b64_tr_b16 v[180:181], v6 offset:15488\n"
        "ds_read_b64_tr_b16 v[182:183], v6 offset:17024\n"
        "s_mov_b32 exec_lo, 0xf0000f\n"
        "s_mov_b32 exec_hi, 0xf0000f\n"
        "ds_read_b128 v[112:115], v10 offset:512\n"
        "ds_read_b128 v[116:119], v10 offset:640\n"
        "s_nop 0\n"
        "s_mov_b64 exec, -1\n"
        "v_pk_mul_f32 v[80:81], v[80:81], v[128:129]\n"
        "v_pk_mul_f32 v[82:83], v[82:83], v[130:131]\n"
        "v_pk_mul_f32 v[84:85], v[84:85], v[132:133]\n"
        "v_pk_mul_f32 v[86:87], v[86:87], v[134:135]\n"
        "v_pk_mul_f32 v[88:89], v[88:89], v[136:137]\n"
        "v_pk_mul_f32 v[90:91], v[90:91], v[138:139]\n"
        "v_pk_mul_f32 v[92:93], v[92:93], v[140:141]\n"
        "v_pk_mul_f32 v[94:95], v[94:95], v[142:143]\n"
        "v_pk_fma_f32 v[96:97], v[96:97], s[56:57], s[56:57] op_sel_hi:[1,0,0]\n"
        "v_pk_fma_f32 v[98:99], v[98:99], s[56:57], s[56:57] op_sel_hi:[1,0,0]\n"
        "v_pk_fma_f32 v[100:101], v[100:101], s[56:57], s[56:57] op_sel_hi:[1,0,0]\n"
        "v_pk_fma_f32 v[102:103], v[102:103], s[56:57], s[56:57] op_sel_hi:[1,0,0]\n"
        "v_pk_fma_f32 v[104:105], v[104:105], s[56:57], s[56:57] op_sel_hi:[1,0,0]\n"
        "v_pk_fma_f32 v[106:107], v[106:107], s[56:57], s[56:57] op_sel_hi:[1,0,0]\n"
        "v_pk_fma_f32 v[108:109], v[108:109], s[56:57], s[56:57] op_sel_hi:[1,0,0]\n"
        "v_pk_fma_f32 v[110:111], v[110:111], s[56:57], s[56:57] op_sel_hi:[1,0,0]\n"
        "v_mfma_f32_16x16x32_bf16 v[64:67], v[184:187], v[120:123], v[64:67]\n"
        "v_cvt_pk_bf16_f32 v40, v80, v81\n"
        "v_cvt_pk_bf16_f32 v41, v82, v83\n"
        "v_and_b32_e64 v55, 3, v0\n"
        "v_mfma_f32_16x16x32_bf16 v[68:71], v[188:191], v[120:123], v[68:71]\n"
        "v_cvt_pk_bf16_f32 v42, v84, v85\n"
        "v_cvt_pk_bf16_f32 v43, v86, v87\n"
        "v_rcp_f32_e64 v96, v96\n"
        "v_mul_u32_u24 v12, 192, v55\n"
        "v_mfma_f32_16x16x32_bf16 v[72:75], v[192:195], v[120:123], v[72:75]\n"
        "v_cvt_pk_bf16_f32 v44, v88, v89\n"
        "v_cvt_pk_bf16_f32 v45, v90, v91\n"
        "v_and_b32_e64 v55, 16, v0\n"
        "v_mfma_f32_16x16x32_bf16 v[64:67], v[196:199], v[124:127], v[64:67]\n"
        "v_cvt_pk_bf16_f32 v46, v92, v93\n"
        "v_cvt_pk_bf16_f32 v47, v94, v95\n"
        "v_rcp_f32_e64 v97, v97\n"
        "v_lshl_add_u32 v12, v55, 1, v12\n"
        "v_mfma_f32_16x16x32_bf16 v[68:71], v[200:203], v[124:127], v[68:71]\n"
        "v_rcp_f32 v98, v98\n"
        "v_and_b32 v55, 4, v0\n"
        "v_mfma_f32_16x16x32_bf16 v[72:75], v[204:207], v[124:127], v[72:75]\n"
        "v_rcp_f32 v99, v99\n"
        "v_rcp_f32 v100, v100\n"
        "v_lshl_add_u32 v12, v55, 2, v12\n"
        "s_waitcnt lgkmcnt(0)\n"
        "v_mfma_f32_16x16x32_bf16 v[64:67], v[160:163], v[112:115], v[64:67]\n"
        "v_rcp_f32 v101, v101\n"
        "v_bfe_u32 v55, v0, 5, 1\n"
        "v_mfma_f32_16x16x32_bf16 v[68:71], v[164:167], v[112:115], v[68:71]\n"
        "v_rcp_f32 v102, v102\n"
        "v_rcp_f32 v103, v103\n"
        "v_lshl_add_u32 v12, v55, 3, v12\n"
        "v_mfma_f32_16x16x32_bf16 v[72:75], v[168:171], v[112:115], v[72:75]\n"
        "v_rcp_f32 v104, v104\n"
        "s_sub_u32 s46, s20, 4\n"
        "v_mfma_f32_16x16x32_bf16 v[64:67], v[172:175], v[116:119], v[64:67]\n"
        "v_rcp_f32 v105, v105\n"
        "v_rcp_f32 v106, v106\n"
        "s_mul_i32 s46, s46, 768\n"
        "v_mfma_f32_16x16x32_bf16 v[68:71], v[176:179], v[116:119], v[68:71]\n"
        "v_rcp_f32_e64 v107, v107\n"
        "s_add_u32 s46, s46, 159744\n"
        "v_mfma_f32_16x16x32_bf16 v[72:75], v[180:183], v[116:119], v[72:75]\n"
        "v_rcp_f32 v108, v108\n"
        "v_rcp_f32 v109, v109\n"
        "v_add_u32_e64 v12, s46, v12\n"
        "v_mfma_f32_32x32x16_bf16 v[208:223], v[184:187], v[40:43], v[208:223]\n"
        "v_rcp_f32 v110, v110\n"
        "v_rcp_f32 v111, v111\n"
        "v_mfma_f32_32x32x16_bf16 v[224:239], v[188:191], v[40:43], v[224:239]\n"
        "v_mfma_f32_32x32x16_bf16 v[240:255], v[192:195], v[40:43], v[240:255]\n"
        "v_mfma_f32_32x32x16_bf16 v[208:223], v[196:199], v[44:47], v[208:223]\n"
        "v_mfma_f32_32x32x16_bf16 v[224:239], v[200:203], v[44:47], v[224:239]\n"
        "v_mfma_f32_32x32x16_bf16 v[240:255], v[204:207], v[44:47], v[240:255]\n"
        "v_cvt_pk_bf16_f32 v80, v64, v65\n"
        "v_cvt_pk_bf16_f32 v81, v66, v67\n"
        "v_cvt_pk_bf16_f32 v82, v68, v69\n"
        "v_cvt_pk_bf16_f32 v83, v70, v71\n"
        "v_cvt_pk_bf16_f32 v84, v72, v73\n"
        "v_cvt_pk_bf16_f32 v85, v74, v75\n"
        "s_mov_b32 exec_lo, 0xff00ff\n"
        "s_mov_b32 exec_hi, 0xff00ff\n"
        "ds_write_b64 v12, v[80:81] offset:0\n"
        "ds_write_b64 v12, v[82:83] offset:64\n"
        "ds_write_b64 v12, v[84:85] offset:128\n"
        "s_mov_b64 exec, 1\n"
        "ds_add_u32 v52, v53 offset:12\n"
        "s_mov_b64 exec, -1\n"
        "v_pk_mul_f32 v[96:97], v[96:97], v[144:145]\n"
        "v_pk_mul_f32 v[98:99], v[98:99], v[146:147]\n"
        "v_pk_mul_f32 v[100:101], v[100:101], v[148:149]\n"
        "v_pk_mul_f32 v[102:103], v[102:103], v[150:151]\n"
        "v_pk_mul_f32 v[104:105], v[104:105], v[152:153]\n"
        "v_pk_mul_f32 v[106:107], v[106:107], v[154:155]\n"
        "v_pk_mul_f32 v[108:109], v[108:109], v[156:157]\n"
        "v_pk_mul_f32 v[110:111], v[110:111], v[158:159]\n"
        "v_cvt_pk_bf16_f32 v40, v96, v97\n"
        "v_cvt_pk_bf16_f32 v41, v98, v99\n"
        "v_cvt_pk_bf16_f32 v42, v100, v101\n"
        "v_cvt_pk_bf16_f32 v43, v102, v103\n"
        "v_cvt_pk_bf16_f32 v44, v104, v105\n"
        "v_cvt_pk_bf16_f32 v45, v106, v107\n"
        "v_cvt_pk_bf16_f32 v46, v108, v109\n"
        "v_cvt_pk_bf16_f32 v47, v110, v111\n"
        "s_nop 0\n"
        "s_nop 0\n"
        "v_mfma_f32_32x32x16_bf16 v[208:223], v[160:163], v[40:43], v[208:223]\n"
        "v_mfma_f32_32x32x16_bf16 v[208:223], v[172:175], v[44:47], v[208:223]\n"
        "v_mfma_f32_32x32x16_bf16 v[224:239], v[164:167], v[40:43], v[224:239]\n"
        "v_mfma_f32_32x32x16_bf16 v[224:239], v[176:179], v[44:47], v[224:239]\n"
        "s_nop 5\n"
        "v_mfma_f32_32x32x16_bf16 v[240:255], v[168:171], v[40:43], v[240:255]\n"
        "s_nop 2\n"
        "v_cvt_pk_bf16_f32 v128, v208, v209\n"
        "v_cvt_pk_bf16_f32 v129, v210, v211\n"
        "v_mfma_f32_32x32x16_bf16 v[240:255], v[180:183], v[44:47], v[240:255]\n"
        "v_cvt_pk_bf16_f32 v130, v212, v213\n"
        "v_cvt_pk_bf16_f32 v131, v214, v215\n"
        "v_cvt_pk_bf16_f32 v132, v216, v217\n"
        "v_cvt_pk_bf16_f32 v133, v218, v219\n"
        "v_cvt_pk_bf16_f32 v134, v220, v221\n"
        "v_cvt_pk_bf16_f32 v135, v222, v223\n"
        "ds_write_b128 v11, v[128:131] offset:0\n"
        "ds_write_b128 v11, v[132:135] offset:1024\n"
        "v_cvt_pk_bf16_f32 v136, v224, v225\n"
        "v_cvt_pk_bf16_f32 v137, v226, v227\n"
        "v_cvt_pk_bf16_f32 v138, v228, v229\n"
        "v_cvt_pk_bf16_f32 v139, v230, v231\n"
        "v_cvt_pk_bf16_f32 v140, v232, v233\n"
        "v_cvt_pk_bf16_f32 v141, v234, v235\n"
        "v_cvt_pk_bf16_f32 v142, v236, v237\n"
        "v_cvt_pk_bf16_f32 v143, v238, v239\n"
        "ds_write_b128 v11, v[136:139] offset:2048\n"
        "ds_write_b128 v11, v[140:143] offset:3072\n"
        "s_mov_b64 exec, 1\n"
        "ds_add_u32 v13, v53 offset:0\n"
        "s_mov_b64 exec, -1\n"
        "ds_read_b32 v54, v13 offset:8\n"
        "s_waitcnt lgkmcnt(0)\n"
        "v_readfirstlane_b32 s23, v54\n"
        "s_cmp_ge_u32 s23, 1\n"
        "s_cbranch_scc0 .L28_%=\n"
        ".L29_%=:\n"
        "ds_read_b128 v[32:35], v11 offset:4096\n"
        "ds_read_b128 v[36:39], v11 offset:5120\n"
        "ds_read_b32 v54, v52 offset:12\n"
        "s_waitcnt lgkmcnt(0)\n"
        "v_mfma_f32_32x32x16_bf16 v[240:255], v[56:59], v[32:35], v[240:255]\n"
        "v_mfma_f32_32x32x16_bf16 v[240:255], v[60:63], v[36:39], v[240:255]\n"
        "s_nop 11\n"
        "v_cvt_pk_bf16_f32 v144, v240, v241\n"
        "v_cvt_pk_bf16_f32 v145, v242, v243\n"
        "v_cvt_pk_bf16_f32 v146, v244, v245\n"
        "v_cvt_pk_bf16_f32 v147, v246, v247\n"
        "v_cvt_pk_bf16_f32 v148, v248, v249\n"
        "v_cvt_pk_bf16_f32 v149, v250, v251\n"
        "v_cvt_pk_bf16_f32 v150, v252, v253\n"
        "v_cvt_pk_bf16_f32 v151, v254, v255\n"
        "s_nop 0\n"
        "v_permlane16_swap_b32 v144, v148\n"
        "v_permlane16_swap_b32 v145, v149\n"
        "v_permlane16_swap_b32 v146, v150\n"
        "v_permlane16_swap_b32 v147, v151\n"
        "buffer_store_dwordx4 v[144:147], v1, s[40:43], 0 offen offset:128 nt\n"
        "buffer_store_dwordx4 v[148:151], v15, s[40:43], 0 offen offset:128 nt\n"
        "s_cmp_eq_u32 s20, 5\n"
        "s_cbranch_scc0 .L30_%=\n"
        "v_readfirstlane_b32 s23, v54\n"
        "s_cmp_ge_u32 s23, 4\n"
        "s_cbranch_scc0 .L31_%=\n"
        ".L32_%=:\n"
        "v_add_u32 v12, 159744, v2\n"
        "s_mov_b32 exec_hi, 0xffff\n"
        "ds_read_b128 v[16:19], v12 offset:0\n"
        "ds_read_b128 v[20:23], v12 offset:768\n"
        "ds_read_b128 v[24:27], v12 offset:1536\n"
        "ds_read_b128 v[28:31], v12 offset:2304\n"
        "s_waitcnt vmcnt(2)\n"
        "v_mov_b64 v[32:33], v[48:49]\n"
        "v_mov_b64 v[34:35], v[50:51]\n"
        "s_waitcnt lgkmcnt(0)\n"
        "v_lshlrev_b32_e64 v64, 16, v16\n"
        "v_and_b32 v65, 0xffff0000, v16\n"
        "v_lshlrev_b32_e64 v66, 16, v17\n"
        "v_and_b32 v67, 0xffff0000, v17\n"
        "v_lshlrev_b32_e64 v68, 16, v18\n"
        "v_and_b32 v69, 0xffff0000, v18\n"
        "v_lshlrev_b32_e64 v70, 16, v19\n"
        "v_and_b32 v71, 0xffff0000, v19\n"
        "v_lshlrev_b32_e64 v72, 16, v20\n"
        "v_and_b32 v73, 0xffff0000, v20\n"
        "v_lshlrev_b32_e64 v74, 16, v21\n"
        "v_and_b32 v75, 0xffff0000, v21\n"
        "v_lshlrev_b32_e64 v76, 16, v22\n"
        "v_and_b32 v77, 0xffff0000, v22\n"
        "v_lshlrev_b32_e64 v78, 16, v23\n"
        "v_and_b32 v79, 0xffff0000, v23\n"
        "v_lshlrev_b32_e64 v80, 16, v24\n"
        "v_and_b32 v81, 0xffff0000, v24\n"
        "v_lshlrev_b32_e64 v82, 16, v25\n"
        "v_and_b32 v83, 0xffff0000, v25\n"
        "v_lshlrev_b32_e64 v84, 16, v26\n"
        "v_and_b32 v85, 0xffff0000, v26\n"
        "v_lshlrev_b32_e64 v86, 16, v27\n"
        "v_and_b32 v87, 0xffff0000, v27\n"
        "v_lshlrev_b32_e64 v88, 16, v28\n"
        "v_and_b32 v89, 0xffff0000, v28\n"
        "v_lshlrev_b32_e64 v90, 16, v29\n"
        "v_and_b32 v91, 0xffff0000, v29\n"
        "v_lshlrev_b32_e64 v92, 16, v30\n"
        "v_and_b32 v93, 0xffff0000, v30\n"
        "v_lshlrev_b32_e64 v94, 16, v31\n"
        "v_and_b32 v95, 0xffff0000, v31\n"
        "v_lshlrev_b32_e64 v96, 16, v32\n"
        "v_and_b32 v97, 0xffff0000, v32\n"
        "v_lshlrev_b32_e64 v98, 16, v33\n"
        "v_and_b32 v99, 0xffff0000, v33\n"
        "v_lshlrev_b32_e64 v100, 16, v34\n"
        "v_and_b32 v101, 0xffff0000, v34\n"
        "v_lshlrev_b32_e64 v102, 16, v35\n"
        "v_and_b32 v103, 0xffff0000, v35\n"
        "v_pk_add_f32 v[64:65], v[64:65], v[72:73]\n"
        "v_pk_add_f32 v[66:67], v[66:67], v[74:75]\n"
        "v_pk_add_f32 v[68:69], v[68:69], v[76:77]\n"
        "v_pk_add_f32 v[70:71], v[70:71], v[78:79]\n"
        "v_pk_add_f32 v[80:81], v[80:81], v[88:89]\n"
        "v_pk_add_f32 v[82:83], v[82:83], v[90:91]\n"
        "v_pk_add_f32 v[84:85], v[84:85], v[92:93]\n"
        "v_pk_add_f32 v[86:87], v[86:87], v[94:95]\n"
        "v_pk_add_f32 v[64:65], v[64:65], v[80:81]\n"
        "v_pk_add_f32 v[66:67], v[66:67], v[82:83]\n"
        "v_pk_add_f32 v[68:69], v[68:69], v[84:85]\n"
        "v_pk_add_f32 v[70:71], v[70:71], v[86:87]\n"
        "v_pk_add_f32 v[64:65], v[64:65], v[96:97]\n"
        "v_pk_add_f32 v[66:67], v[66:67], v[98:99]\n"
        "v_pk_add_f32 v[68:69], v[68:69], v[100:101]\n"
        "v_pk_add_f32 v[70:71], v[70:71], v[102:103]\n"
        "v_cvt_pk_bf16_f32 v16, v64, v65\n"
        "v_cvt_pk_bf16_f32 v17, v66, v67\n"
        "v_cvt_pk_bf16_f32 v18, v68, v69\n"
        "v_cvt_pk_bf16_f32 v19, v70, v71\n"
        "buffer_store_dwordx4 v[16:19], v2, s[40:43], s44 offen nt\n"
        "s_mov_b64 exec, -1\n"
        ".L30_%=:\n"
        ".L27_%=:\n"
        ".L23_%=:\n"
        "s_branch .L13_%=\n"
        ".L16_%=:\n"
        "s_nop 0\n"
        "s_sleep 1\n"
        "ds_read_b32 v54, v52 offset:0\n"
        "s_waitcnt lgkmcnt(0)\n"
        "v_readfirstlane_b32 s23, v54\n"
        "s_cmp_ge_u32 s23, 8\n"
        "s_cbranch_scc0 .L16_%=\n"
        "s_branch .L17_%=\n"
        ".L20_%=:\n"
        "s_sleep 1\n"
        "ds_read_b32 v54, v7 offset:0\n"
        "s_waitcnt lgkmcnt(0)\n"
        "v_readfirstlane_b32 s23, v54\n"
        "s_cmp_ge_u32 s23, 4\n"
        "s_cbranch_scc0 .L20_%=\n"
        "s_branch .L21_%=\n"
        ".L25_%=:\n"
        "s_sleep 1\n"
        "ds_read_b32 v54, v13 offset:0\n"
        "s_waitcnt lgkmcnt(0)\n"
        "v_readfirstlane_b32 s23, v54\n"
        "s_cmp_ge_u32 s23, 1\n"
        "s_cbranch_scc0 .L25_%=\n"
        "s_branch .L26_%=\n"
        ".L28_%=:\n"
        "s_sleep 1\n"
        "ds_read_b32 v54, v13 offset:8\n"
        "s_waitcnt lgkmcnt(0)\n"
        "v_readfirstlane_b32 s23, v54\n"
        "s_cmp_ge_u32 s23, 1\n"
        "s_cbranch_scc0 .L28_%=\n"
        "s_branch .L29_%=\n"
        ".L31_%=:\n"
        "s_sleep 1\n"
        "ds_read_b32 v54, v52 offset:12\n"
        "s_waitcnt lgkmcnt(0)\n"
        "v_readfirstlane_b32 s23, v54\n"
        "s_cmp_ge_u32 s23, 4\n"
        "s_cbranch_scc0 .L31_%=\n"
        "s_branch .L32_%=\n"
        ".L13_%=:\n"
        :: "s"(__builtin_amdgcn_kernarg_segment_ptr()), "s"(blockIdx.x), "v"(threadIdx.x), "s"((unsigned)(size_t)smem)
        : "memory", "scc", "vcc", "v1", "v2", "v3", "v4", "v5", "v6", "v7", "v8", "v9", "v10", "v11", "v12", "v13", "v14", "v15", "v16", "v17", "v18", "v19", "v20", "v21", "v22", "v23", "v24", "v25", "v26", "v27", "v28", "v29", "v30", "v31", "v32", "v33", "v34", "v35", "v36", "v37", "v38", "v39", "v40", "v41", "v42", "v43", "v44", "v45", "v46", "v47", "v48", "v49", "v50", "v51", "v52", "v53", "v54", "v55", "v56", "v57", "v58", "v59", "v60", "v61", "v62", "v63", "v64", "v65", "v66", "v67", "v68", "v69", "v70", "v71", "v72", "v73", "v74", "v75", "v76", "v77", "v78", "v79", "v80", "v81", "v82", "v83", "v84", "v85", "v86", "v87", "v88", "v89", "v90", "v91", "v92", "v93", "v94", "v95", "v96", "v97", "v98", "v99", "v100", "v101", "v102", "v103", "v104", "v105", "v106", "v107", "v108", "v109", "v110", "v111", "v112", "v113", "v114", "v115", "v116", "v117", "v118", "v119", "v120", "v121", "v122", "v123", "v124", "v125", "v126", "v127", "v128", "v129", "v130", "v131", "v132", "v133", "v134", "v135", "v136", "v137", "v138", "v139", "v140", "v141", "v142", "v143", "v144", "v145", "v146", "v147", "v148", "v149", "v150", "v151", "v152", "v153", "v154", "v155", "v156", "v157", "v158", "v159", "v160", "v161", "v162", "v163", "v164", "v165", "v166", "v167", "v168", "v169", "v170", "v171", "v172", "v173", "v174", "v175", "v176", "v177", "v178", "v179", "v180", "v181", "v182", "v183", "v184", "v185", "v186", "v187", "v188", "v189", "v190", "v191", "v192", "v193", "v194", "v195", "v196", "v197", "v198", "v199", "v200", "v201", "v202", "v203", "v204", "v205", "v206", "v207", "v208", "v209", "v210", "v211", "v212", "v213", "v214", "v215", "v216", "v217", "v218", "v219", "v220", "v221", "v222", "v223", "v224", "v225", "v226", "v227", "v228", "v229", "v230", "v231", "v232", "v233", "v234", "v235", "v236", "v237", "v238", "v239", "v240", "v241", "v242", "v243", "v244", "v245", "v246", "v247", "v248", "v249", "v250", "v251", "v252", "v253", "v254", "v255", "s4", "s5", "s6", "s7", "s8", "s9", "s10", "s11", "s12", "s13", "s14", "s15", "s16", "s17", "s18", "s19", "s20", "s21", "s22", "s23", "s24", "s25", "s26", "s27", "s28", "s29", "s30", "s31", "s36", "s37", "s38", "s39", "s40", "s41", "s42", "s43", "s44", "s45", "s46", "s47", "s48", "s49", "s50", "s51", "s52", "s53", "s54", "s55", "s56", "s57", "s58", "s59", "s60", "s61", "s62", "s63", "s64", "s65", "s66", "s67", "s68", "s69", "s70", "s71", "s72", "s73", "s74", "s75", "s76", "s77", "s78", "s79", "s80", "s81", "s82", "s83", "s84", "s85", "s86", "s87", "s88", "s89", "s90", "s91", "s92", "s93", "s94", "s95", "s96", "s97", "s98", "s99");
}

extern "C" void kernel_launch(void* const* d_in, const int* in_sizes, int n_in, void* d_out, int out_size, void* d_ws, size_t ws_size,
                              hipStream_t stream) {
    convnext_kernel<<<256, 512, 0, stream>>>((const char*)d_in[0], (const char*)d_in[1], (const char*)d_in[2], (const char*)d_in[3], (char*)d_out);
}
```
